```python
import torch

def _softmax(x, axis=-1):
    return torch.softmax(x, dim=axis)

def _gelu(x):
    return torch.nn.functional.gelu(x, approximate='tanh')

def _silu(x):
    return torch.nn.functional.silu(x)

def _sigmoid(x):
    return torch.sigmoid(x)

def _layernorm(x, axis=-1, eps=1e-05):
    m = x.mean(dim=axis, keepdim=True)
    v = x.var(dim=axis, keepdim=True, unbiased=False)
    return (x - m) / torch.sqrt(v + eps)

def _scatter_add(tgt, idx, src, axis=0):
    return tgt.clone().index_add_(axis, idx, src)

def _segment_sum(src, seg, n):
    out = torch.zeros((n,) + src.shape[1:], dtype=src.dtype)
    return out.index_add_(0, seg, src)

def _pad(x, pw):
    flat = tuple((v for pair in reversed(pw) for v in pair))
    return torch.nn.functional.pad(x, flat)
kernel_name = 'standalone__autoregressive_conv_wavenet__speech'

def setup_inputs():

    _w = lambda a: (lambda t: t.to(torch.bfloat16) if t.is_floating_point() else t)(torch.as_tensor(a))
    B, T, D, K = (2, 1000, 144, 31)
    return {'x': _w((lambda *s: torch.randn(*s, dtype=torch.bfloat16) * 0.1)(B, T, D)), 'w': _w((lambda *s: torch.randn(*s, dtype=torch.bfloat16) * 0.1)(K, D, 2 * D)), 'w_o': _w((lambda *s: torch.randn(*s, dtype=torch.bfloat16) * 0.05)(D, D))}

def reference(x, w, w_o):
    B, T, D = x.shape
    K = w.shape[0]
    dil = 2
    pad = (K - 1) * dil
    xp = _pad(x, ((0, 0), (pad, 0), (0, 0)))
    h = torch.einsum('btc,cd->btd', xp[:, pad:pad + T, :], w[0])
    for i in range(1, K):
        off = pad - i * dil
        h = h + torch.einsum('btc,cd->btd', xp[:, off:off + T, :], w[i])
    f = h[..., :D].clone()
    g = h[..., D:].clone()
    z = torch.tanh(f) * _sigmoid(g)
    return z @ w_o
```

```cpp
#include <hip/hip_runtime.h>
#include <hip/hip_bf16.h>
#define B_ 2
#define T_ 1000
#define D_ 144
#define K_ 31
__device__ inline float bf2f(unsigned short u){ return __uint_as_float(((unsigned)u)<<16); }
__global__ void k1(const unsigned short* x, const unsigned short* w, float* z){
  int row = blockIdx.x; int j = threadIdx.x; if (j>=D_) return;
  int b = row / T_, t = row % T_;
  float f=0.f,g=0.f;
  for (int i=0;i<K_;i++){ int tt=t-2*i; if(tt<0) break;
    const unsigned short* xr = x + (size_t)(b*T_+tt)*D_;
    const unsigned short* wr = w + (size_t)i*D_*2*D_;
    for(int c=0;c<D_;c++){ float xv=bf2f(xr[c]); f+=xv*bf2f(wr[c*2*D_+j]); g+=xv*bf2f(wr[c*2*D_+D_+j]); }
  }
  z[row*D_+j] = tanhf(f)/(1.f+__expf(-g));
}
__global__ void k2(const float* z, const unsigned short* wo, __hip_bfloat16* out){
  int row = blockIdx.x; int j = threadIdx.x; if (j>=D_) return;
  float a=0.f;
  for(int c=0;c<D_;c++) a += z[row*D_+c]*bf2f(wo[c*D_+j]);
  out[row*D_+j] = __float2bfloat16(a);
}
extern "C" void kernel_launch(void* const* d_in, const int* in_sizes, int n_in,
                              void* d_out, int out_size, void* d_ws, size_t ws_size,
                              hipStream_t stream) {
  k1<<<B_*T_,192,0,stream>>>((const unsigned short*)d_in[0],(const unsigned short*)d_in[1],(float*)d_ws);
  k2<<<B_*T_,192,0,stream>>>((const float*)d_ws,(const unsigned short*)d_in[2],(__hip_bfloat16*)d_out);
}
```

```cpp
#include <hip/hip_runtime.h>
#include <hip/hip_bf16.h>

typedef unsigned long long u64;
typedef __attribute__((ext_vector_type(8))) short s16x8;
typedef __attribute__((ext_vector_type(4))) short s16x4;
typedef __attribute__((ext_vector_type(4))) float f32x4;
typedef __attribute__((ext_vector_type(2))) float f32x2;
typedef __attribute__((ext_vector_type(4))) unsigned u32x4;
typedef __attribute__((ext_vector_type(2))) unsigned u32x2;
typedef __attribute__((ext_vector_type(2))) __bf16 bf16x2;
#define LDSP(T, a) ((__attribute__((address_space(3))) T*)(size_t)(a))

#define RLX_AGENT __ATOMIC_RELAXED, __HIP_MEMORY_SCOPE_AGENT
typedef __attribute__((address_space(1))) u64 gu64;

constexpr int W_BYTES = 31 * 144 * 288 * 2;
constexpr int X_BYTES = 2000 * 144 * 2;
constexpr int WO_BYTES = 144 * 144 * 2;
constexpr int L_W = 0;
constexpr int L_X = 98304;
constexpr int L_FLAG = L_X + 290 * 96;
constexpr int L_Z = L_FLAG + 64;
constexpr int L_END = L_Z + 8 * 336;
constexpr int WS_EPOCH = 0;
constexpr int WS_FLAG = 2048;
constexpr int WS_PART = 65536;
constexpr int PART_BYTES = 9 * 28 * 27 * 512;

constexpr int SPIN_MAX = 1 << 20;
#define STAMP(i)

__device__ __forceinline__ unsigned pk_bf16(float a, float b) {
  f32x2 v = {a, b};
  bf16x2 r = __builtin_convertvector(v, bf16x2);
  return __builtin_bit_cast(unsigned, r);
}
__device__ __forceinline__ float bf_lo(unsigned u) { return __builtin_bit_cast(float, u << 16); }
__device__ __forceinline__ float bf_hi(unsigned u) { return __builtin_bit_cast(float, u & 0xffff0000u); }

__device__ __forceinline__ s16x8 tr_frag(unsigned a) {
  s16x4 lo = __builtin_amdgcn_ds_read_tr16_b64_v4i16(LDSP(s16x4, a));
  s16x4 hi = __builtin_amdgcn_ds_read_tr16_b64_v4i16(LDSP(s16x4, a + 256));
  return __builtin_shufflevector(lo, hi, 0, 1, 2, 3, 4, 5, 6, 7);
}
__device__ __forceinline__ s16x8 ld_frag(unsigned a) { return *LDSP(s16x8, a); }
#define MFMA(a, b, c) __builtin_amdgcn_mfma_f32_16x16x32_bf16(a, b, c, 0, 0, 0)

__device__ __forceinline__ float gate(float f, float g) {
  f = __builtin_amdgcn_fmed3f(f, -20.f, 20.f);
  float E = __builtin_amdgcn_exp2f(f * 2.885390081777927f);
  float G = __builtin_amdgcn_exp2f(g * -1.4426950408889634f);
  return (E - 1.f) * __builtin_amdgcn_rcpf((E + 1.f) * (1.f + G));
}

__global__ __launch_bounds__(512, 2) void wavenet_kernel(const char* __restrict__ x, const char* __restrict__ w,
                                                         const char* __restrict__ wo, char* __restrict__ out,
                                                         char* __restrict__ ws
) {
  __shared__ __attribute__((aligned(1024))) char shm[L_END];
  const unsigned lds0 = (unsigned)(size_t)(__attribute__((address_space(3))) char*)shm;
  STAMP(0);
  const int lane = threadIdx.x & 63;
  const int wid = __builtin_amdgcn_readfirstlane(threadIdx.x >> 6);
  const int b = blockIdx.x;
  const int xcd = b & 7;
  const int id = xcd * 31 + min(xcd, 4) + (b >> 3);
  const int c = id / 9, m = id - c * 9;
  const bool producer = c < 27;
  const int g = c / 9, n = c - g * 9;
  const int R0 = 32 * m;
  const int sA = R0 / 72;
  const int rA = R0 - sA * 72;
  const int blk0 = m & 7;
  u64 T;

  if (wid >= 4) {
    const int lw = wid - 4;
    if (lane == 0) *LDSP(volatile unsigned, lds0 + L_FLAG + lw * 4) = 0;
    auto rw = __builtin_amdgcn_make_buffer_rsrc((void*)w, 0, W_BYTES, 0x00020000);
    auto rx = __builtin_amdgcn_make_buffer_rsrc((void*)x, 0, X_BYTES, 0x00020000);
    auto rwo = __builtin_amdgcn_make_buffer_rsrc((void*)wo, 0, WO_BYTES, 0x00020000);
    if (producer) {
      const int wrow = lane >> 2;
      const unsigned wl = (g * 48 + wrow) * 576 + n * 64 + (((lane & 3) ^ ((wrow >> 3) << 1)) << 4);
      const unsigned o0 = ((lw >> 1) * 144 + (lw & 1) * 16) * 576;
      const unsigned o1 = o0 + 2 * 144 * 576;
      const unsigned o2 = (lw * 144 + 32) * 576;
      auto wblock = [&](int ord) {
        const unsigned tb = (((blk0 + ord) & 7) * 4 * 144 * 576);
        const unsigned dst = lds0 + L_W + ord * 12288 + lw * 1024;
        __builtin_amdgcn_raw_ptr_buffer_load_lds(rw, LDSP(void, dst), 16, wl + (tb + o0), 0, 0, 0);
        __builtin_amdgcn_raw_ptr_buffer_load_lds(rw, LDSP(void, dst + 4096), 16, wl + (tb + o1), 0, 0, 0);
        __builtin_amdgcn_raw_ptr_buffer_load_lds(rw, LDSP(void, dst + 8192), 16, wl + (tb + o2), 0, 0, 0);
      };
      wblock(0);
      const int xr = (lane * 43) >> 8;
      const int xc = lane - xr * 6;
      const int nx = (rA + 32 > 72) ? 29 : 26;
      const int baseA = ((sA >> 1) * 1000 + (sA & 1)) * 288 + g * 96 + xc * 16;
      const int sB = sA + 1;
      const int baseB = ((sB >> 1) * 1000 + (sB & 1)) * 288 + g * 96 + xc * 16;
      const int p0 = rA * 7 - 31 + xr;
      int k = (n % 3) * 9 + lw;
      if (lane < 60) {
        for (int kk = lw; kk < nx; kk += 4) {
          if (k >= nx) k -= nx;
          int p = p0 + k * 10;
          const bool second = p >= 504;
          p = second ? p - 536 : p;
          unsigned off = (second ? baseB : baseA) + p * 576;
          off = ((unsigned)p < 500u) ? off : 0x80000000u;
          __builtin_amdgcn_raw_ptr_buffer_load_lds(rx, LDSP(void, lds0 + L_X + k * 960), 16, off, 0, 0, 0);
          k += 4;
        }
      }
      asm volatile("s_waitcnt lgkmcnt(0)" ::: "memory");
      __builtin_amdgcn_s_barrier();
      STAMP(1);
      for (int ord = 1; ord < 8; ++ord) {
        wblock(ord);
        asm volatile("s_waitcnt vmcnt(3)" ::: "memory");
        if (lane == 0) *LDSP(volatile unsigned, lds0 + L_FLAG + lw * 4) = ord;
      }
      asm volatile("s_waitcnt vmcnt(0)" ::: "memory");
      if (lane == 0) *LDSP(volatile unsigned, lds0 + L_FLAG + lw * 4) = 8;
      STAMP(2);
      for (int sp = 0; sp < SPIN_MAX; ++sp) {
        u32x4 v = *LDSP(volatile u32x4, lds0 + L_FLAG + 16);
        if (__builtin_amdgcn_readfirstlane(v[0] & v[1] & v[2] & v[3]) == 1) break;
        __builtin_amdgcn_s_sleep(2);
      }
    } else {
      asm volatile("s_waitcnt lgkmcnt(0)" ::: "memory");
      __builtin_amdgcn_s_barrier();
    }
    for (int kk = lw; kk < 41; kk += 4)
      __builtin_amdgcn_raw_ptr_buffer_load_lds(rwo, LDSP(void, lds0 + L_W + kk * 1024), 16, kk * 1024 + lane * 16, 0, 0, 0);
    T = __hip_atomic_load((gu64*)(ws + WS_EPOCH + m * 128), RLX_AGENT) * 0x9E3779B97F4A7C15ull + 0x632BE59BD9B4E019ull;
    asm volatile("s_waitcnt vmcnt(0)" ::: "memory");
    STAMP(3);
  } else {
    if (lane == 0) *LDSP(volatile unsigned, lds0 + L_FLAG + 16 + wid * 4) = 0;
    T = __hip_atomic_load((gu64*)(ws + WS_EPOCH + m * 128), RLX_AGENT) * 0x9E3779B97F4A7C15ull + 0x632BE59BD9B4E019ull;
    asm volatile("s_waitcnt lgkmcnt(0)" ::: "memory");
    __builtin_amdgcn_s_barrier();
    if (producer) {
      const int mh = wid >> 1, nh = wid & 1;
      const int r = lane & 15, q = lane >> 4;
      const int rr = 16 * mh + r;
      const int lR = 31 + 7 * rr + ((rA + rr >= 72) ? 32 : 0);
      const unsigned xa0 = lds0 + L_X + (lR - 3) * 96 + 16 * q;
      const unsigned xb0 = lds0 + L_X + (lR - 2 - (q >> 1)) * 96 + 64 + 16 * (q & 1);
      unsigned wa = lds0 + L_W + (8 * q + (r >> 2)) * 64 + ((nh ^ (q & 1)) << 5) + (lane & 3) * 8;
      f32x4 acc[7];
#pragma unroll
      for (int j = 0; j < 7; ++j) acc[j] = f32x4{0.f, 0.f, 0.f, 0.f};
      auto wait_stage = [&](unsigned s) {
        for (int sp = 0; sp < SPIN_MAX; ++sp) {
          u32x4 v = *LDSP(volatile u32x4, lds0 + L_FLAG);
          unsigned mn = min(min(v[0], v[1]), min(v[2], v[3]));
          if (__builtin_amdgcn_readfirstlane(mn) >= s) break;
        }
      };
      wait_stage(1);
      STAMP(1);
      s16x8 xa[10], xb[9], wf0, wf1;
      int blk = blk0;
      unsigned xa_ = xa0 - blk * 384, xb_ = xb0 - blk * 384;
#pragma unroll
      for (int d = 3; d < 10; ++d) xa[d] = ld_frag(xa_ + d * 96);
      wf0 = tr_frag(wa);
      for (int ord = 0; ord < 8; ++ord) {
        const int nblk = (blk + 1) & 7;
        const unsigned nxa = xa0 - nblk * 384;
        wf1 = tr_frag(wa + 2048);
        xa[2] = ld_frag(xa_ + 2 * 96);
        xb[2] = ld_frag(xb_ + 2 * 96);
        xb[3] = ld_frag(xb_ + 3 * 96);
#pragma unroll
        for (int j = 0; j < 7; ++j) acc[j] = MFMA(wf0, xa[j + 3], acc[j]);
        __builtin_amdgcn_sched_barrier(0);
        wf0 = tr_frag(wa + 2 * 2048);
        xa[1] = ld_frag(xa_ + 1 * 96);
        xb[4] = ld_frag(xb_ + 4 * 96);
        xb[5] = ld_frag(xb_ + 5 * 96);
#pragma unroll
        for (int j = 0; j < 7; ++j) acc[j] = MFMA(wf1, xa[j + 2], acc[j]);
        __builtin_amdgcn_sched_barrier(0);
        wf1 = tr_frag(wa + 3 * 2048);
        xa[0] = ld_frag(xa_);
        xb[6] = ld_frag(xb_ + 6 * 96);
        xb[7] = ld_frag(xb_ + 7 * 96);
#pragma unroll
        for (int j = 0; j < 7; ++j) acc[j] = MFMA(wf0, xa[j + 1], acc[j]);
        __builtin_amdgcn_sched_barrier(0);
        wf0 = tr_frag(wa + 4 * 2048);
        xb[8] = ld_frag(xb_ + 8 * 96);
        xb[0] = ld_frag(xb_);
        xb[1] = ld_frag(xb_ + 96);
        xa[9] = ld_frag(nxa + 9 * 96);
#pragma unroll
        for (int j = 0; j < 7; ++j) acc[j] = MFMA(wf1, xa[j], acc[j]);
        __builtin_amdgcn_sched_barrier(0);
        wf1 = tr_frag(wa + 5 * 2048);
        xa[8] = ld_frag(nxa + 8 * 96);
        xa[7] = ld_frag(nxa + 7 * 96);
        xa[3] = ld_frag(nxa + 3 * 96);
#pragma unroll
        for (int j = 0; j < 7; ++j) acc[j] = MFMA(wf0, xb[j + 2], acc[j]);
        __builtin_amdgcn_sched_barrier(0);
        if (ord == 3) { asm volatile("s_waitcnt lgkmcnt(0)" ::: "memory"); if (lane == 0) *LDSP(volatile unsigned, lds0 + L_FLAG + 16 + wid * 4) = 1; }
        if (ord < 7) wait_stage(ord + 2);
        wa += 12288;
        wf0 = tr_frag(ord < 7 ? wa : wa - 12288);
        xa[4] = ld_frag(nxa + 4 * 96);
        xa[5] = ld_frag(nxa + 5 * 96);
        xa[6] = ld_frag(nxa + 6 * 96);
#pragma unroll
        for (int j = 0; j < 7; ++j) acc[j] = MFMA(wf1, xb[j], acc[j]);
        __builtin_amdgcn_sched_barrier(0);
        blk = nblk;
        xa_ = nxa;
        xb_ = xb0 - nblk * 384;
      }
      STAMP(2);
      auto rp = __builtin_amdgcn_make_buffer_rsrc((void*)(ws + WS_PART), 0, PART_BYTES, 0x00020000);
      unsigned pk[7][2];
#pragma unroll
      for (int j = 0; j < 7; ++j) { pk[j][0] = pk_bf16(acc[j][0], acc[j][1]); pk[j][1] = pk_bf16(acc[j][2], acc[j][3]); }
      const unsigned pbase = ((m * 28 + 2 * mh + (r >> 3)) * 27 + c) * 512 + nh * 256 + (r & 7) * 32;
#pragma unroll
      for (int jp = 0; jp < 3; ++jp) {
        auto s0 = __builtin_amdgcn_permlane16_swap(pk[2 * jp][0], pk[2 * jp + 1][0], false, false);
        auto s1 = __builtin_amdgcn_permlane16_swap(pk[2 * jp][1], pk[2 * jp + 1][1], false, false);
        u32x4 v = {s0[0], s1[0], s0[1], s1[1]};
        __builtin_amdgcn_raw_buffer_store_b128(v, rp, pbase + (2 * jp + (q & 1)) * (4 * 27 * 512) + (q >> 1) * 16, 0, 16);
      }
      {
        u32x2 v = {pk[6][0], pk[6][1]};
        __builtin_amdgcn_raw_buffer_store_b64(v, rp, pbase + 6 * (4 * 27 * 512) + q * 8, 0, 16);
      }
      asm volatile("s_waitcnt vmcnt(0)" ::: "memory");
      STAMP(3);
    }
  }
  asm volatile("s_waitcnt lgkmcnt(0)" ::: "memory");
  __builtin_amdgcn_s_barrier();
  if (producer && threadIdx.x == 0) __hip_atomic_store((gu64*)(ws + WS_FLAG + (m * 27 + c) * 128), T, RLX_AGENT);
  STAMP(4);
  const int q = lane >> 4;
  s16x8 of[5], of8[5];
  {
    const unsigned a0 = lds0 + L_W + (8 * q + ((lane & 15) >> 2)) * 288 + (lane & 3) * 8;
    const unsigned a4 = a0 + 128 * 288 - (q >> 1) * 16 * 288;
    auto trw = [&](unsigned a) {
      s16x4 lo = __builtin_amdgcn_ds_read_tr16_b64_v4i16(LDSP(s16x4, a));
      s16x4 hi = __builtin_amdgcn_ds_read_tr16_b64_v4i16(LDSP(s16x4, a + 4 * 288));
      return (s16x8)__builtin_shufflevector(lo, hi, 0, 1, 2, 3, 4, 5, 6, 7);
    };
#pragma unroll
    for (int s = 0; s < 4; ++s) { of[s] = trw(a0 + s * 32 * 288 + wid * 32); of8[s] = trw(a0 + s * 32 * 288 + 256); }
    of[4] = trw(a4 + wid * 32);
    of8[4] = trw(a4 + 256);
  }
  if (threadIdx.x >= 448 && threadIdx.x < 464) {
    const int t = threadIdx.x - 448;
    *LDSP(u32x4, lds0 + L_Z + (t >> 1) * 336 + 288 + (t & 1) * 16) = u32x4{0, 0, 0, 0};
  }
  if (wid < 5) {
    gu64* fl = (gu64*)(ws + WS_FLAG + (m * 27 + min(lane, 26)) * 128);
    for (int sp = 0; sp < SPIN_MAX; ++sp) {
      u64 v = __hip_atomic_load(fl, RLX_AGENT);
      if (__all(v == T)) break;
      __builtin_amdgcn_s_sleep(1);
    }
    STAMP(5);
    if (!producer && threadIdx.x == 0) __hip_atomic_store((gu64*)(ws + WS_EPOCH + m * 128), T, RLX_AGENT);
    asm volatile("" ::: "memory");
    if (threadIdx.x < 288) {
      auto rp = __builtin_amdgcn_make_buffer_rsrc((void*)(ws + WS_PART), 0, PART_BYTES, 0x00020000);
      const int t = threadIdx.x;
      const int rho = (t >> 2) & 7;
      const int cq = (t >> 5) * 4 + (t & 3);
      const unsigned cb = (m * 28 + c) * 27 * 512 + rho * 32 + (t & 3) * 8;
      const unsigned fo = cb + (t >> 5) * 256;
      const unsigned go = cb + (9 + (t >> 5)) * 256;
      u32x2 f0 = __builtin_amdgcn_raw_buffer_load_b64(rp, fo, 0, 16);
      u32x2 f1 = __builtin_amdgcn_raw_buffer_load_b64(rp, fo + 9 * 512, 0, 16);
      u32x2 f2 = __builtin_amdgcn_raw_buffer_load_b64(rp, fo + 18 * 512, 0, 16);
      u32x2 g0 = __builtin_amdgcn_raw_buffer_load_b64(rp, go, 0, 16);
      u32x2 g1 = __builtin_amdgcn_raw_buffer_load_b64(rp, go + 9 * 512, 0, 16);
      u32x2 g2 = __builtin_amdgcn_raw_buffer_load_b64(rp, go + 18 * 512, 0, 16);
      u32x2 z;
#pragma unroll
      for (int e = 0; e < 2; ++e) {
        float zl = gate(bf_lo(f0[e]) + bf_lo(f1[e]) + bf_lo(f2[e]), bf_lo(g0[e]) + bf_lo(g1[e]) + bf_lo(g2[e]));
        float zh = gate(bf_hi(f0[e]) + bf_hi(f1[e]) + bf_hi(f2[e]), bf_hi(g0[e]) + bf_hi(g1[e]) + bf_hi(g2[e]));
        z[e] = pk_bf16(zl, zh);
      }
      *LDSP(u32x2, lds0 + L_Z + rho * 336 + cq * 8) = z;
    }
    STAMP(6);
  }
  asm volatile("s_waitcnt lgkmcnt(0)" ::: "memory");
  __builtin_amdgcn_s_barrier();
  {
    const int rho = lane & 7;
    const unsigned za = lds0 + L_Z + rho * 336 + q * 16;
    s16x8 zf[5];
#pragma unroll
    for (int s = 0; s < 5; ++s) zf[s] = ld_frag(za + s * 64);
    f32x4 o = {0.f, 0.f, 0.f, 0.f}, o8 = {0.f, 0.f, 0.f, 0.f};
#pragma unroll
    for (int s = 0; s < 5; ++s) o = MFMA(of[s], zf[s], o);
    if (wid == 0) {
#pragma unroll
      for (int s = 0; s < 5; ++s) o8 = MFMA(of8[s], zf[s], o8);
    }
    int rr = rA + 8 * (c & 3) + rho;
    int sq = sA;
    if (rr >= 72) { rr -= 72; sq += 1; }
    const int p = rr * 7 + (c >> 2);
    if ((lane & 8) == 0 && p < 500) {
      char* orow = out + (size_t)(((sq >> 1) * 1000 + 2 * p + (sq & 1)) * 288 + q * 8);
      *(u32x2*)(orow + wid * 32) = u32x2{pk_bf16(o[0], o[1]), pk_bf16(o[2], o[3])};
      if (wid == 0) *(u32x2*)(orow + 256) = u32x2{pk_bf16(o8[0], o8[1]), pk_bf16(o8[2], o8[3])};
    }
  }
  STAMP(7);
}

extern "C" void kernel_launch(void* const* d_in, const int* in_sizes, int n_in, void* d_out, int out_size, void* d_ws,
                              size_t ws_size, hipStream_t stream) {
  wavenet_kernel<<<252, 512, 0, stream>>>((const char*)d_in[0], (const char*)d_in[1], (const char*)d_in[2], (char*)d_out,
                                          (char*)d_ws);
}
```

```cpp
#define TPROBE 0
#include <hip/hip_runtime.h>
#include <hip/hip_bf16.h>

typedef unsigned long long u64;
typedef __attribute__((ext_vector_type(8))) short s16x8;
typedef __attribute__((ext_vector_type(4))) short s16x4;
typedef __attribute__((ext_vector_type(4))) float f32x4;
typedef __attribute__((ext_vector_type(2))) float f32x2;
typedef __attribute__((ext_vector_type(4))) unsigned u32x4;
typedef __attribute__((ext_vector_type(2))) unsigned u32x2;
typedef __attribute__((ext_vector_type(2))) __bf16 bf16x2;
#define LDSP(T, a) ((__attribute__((address_space(3))) T*)(size_t)(a))

#define RLX_AGENT __ATOMIC_RELAXED, __HIP_MEMORY_SCOPE_AGENT
typedef __attribute__((address_space(1))) u64 gu64;

constexpr int W_BYTES = 31 * 144 * 288 * 2;
constexpr int X_BYTES = 2000 * 144 * 2;
constexpr int WO_BYTES = 144 * 144 * 2;
constexpr int L_W = 0;
constexpr int L_X = 98304;
constexpr int L_FLAG = L_X + 290 * 96;
constexpr int L_Z = L_FLAG + 64;
constexpr int L_END = L_Z + 8 * 336;
constexpr int WS_EPOCH = 0;
constexpr int WS_FLAG = 2048;
constexpr int WS_PART = 65536;
constexpr int PART_BYTES = 9 * 28 * 27 * 512;

constexpr int SPIN_MAX = 1 << 20;
#define STAMP(i) do { if ((i) == 0) { tp0 = tp1 = __builtin_amdgcn_s_memrealtime(); } else if ((i) == TPROBE) { tp1 = __builtin_amdgcn_s_memrealtime(); } \
   if ((i) == 7) { while (__builtin_amdgcn_s_memrealtime() - tp0 < 1200 + (tp1 - tp0)) __builtin_amdgcn_s_sleep(1); } } while (0)

__device__ __forceinline__ unsigned pk_bf16(float a, float b) {
  f32x2 v = {a, b};
  bf16x2 r = __builtin_convertvector(v, bf16x2);
  return __builtin_bit_cast(unsigned, r);
}
__device__ __forceinline__ float bf_lo(unsigned u) { return __builtin_bit_cast(float, u << 16); }
__device__ __forceinline__ float bf_hi(unsigned u) { return __builtin_bit_cast(float, u & 0xffff0000u); }

__device__ __forceinline__ s16x8 tr_frag(unsigned a) {
  s16x4 lo = __builtin_amdgcn_ds_read_tr16_b64_v4i16(LDSP(s16x4, a));
  s16x4 hi = __builtin_amdgcn_ds_read_tr16_b64_v4i16(LDSP(s16x4, a + 256));
  return __builtin_shufflevector(lo, hi, 0, 1, 2, 3, 4, 5, 6, 7);
}
__device__ __forceinline__ s16x8 ld_frag(unsigned a) { return *LDSP(s16x8, a); }
#define MFMA(a, b, c) __builtin_amdgcn_mfma_f32_16x16x32_bf16(a, b, c, 0, 0, 0)

__device__ __forceinline__ float gate(float f, float g) {
  f = __builtin_amdgcn_fmed3f(f, -20.f, 20.f);
  float E = __builtin_amdgcn_exp2f(f * 2.885390081777927f);
  float G = __builtin_amdgcn_exp2f(g * -1.4426950408889634f);
  return (E - 1.f) * __builtin_amdgcn_rcpf((E + 1.f) * (1.f + G));
}

__global__ __launch_bounds__(512, 2) void wavenet_kernel(const char* __restrict__ x, const char* __restrict__ w,
                                                         const char* __restrict__ wo, char* __restrict__ out,
                                                         char* __restrict__ ws
) {
  __shared__ __attribute__((aligned(1024))) char shm[L_END];
  const unsigned lds0 = (unsigned)(size_t)(__attribute__((address_space(3))) char*)shm;
  u64 tp0, tp1;
  STAMP(0);
  const int lane = threadIdx.x & 63;
  const int wid = __builtin_amdgcn_readfirstlane(threadIdx.x >> 6);
  const int b = blockIdx.x;
  const int xcd = b & 7;
  const int id = xcd * 31 + min(xcd, 4) + (b >> 3);
  const int c = id / 9, m = id - c * 9;
  const bool producer = c < 27;
  const int g = c / 9, n = c - g * 9;
  const int R0 = 32 * m;
  const int sA = R0 / 72;
  const int rA = R0 - sA * 72;
  const int blk0 = m & 7;
  u64 T;

  if (wid >= 4) {
    const int lw = wid - 4;
    if (lane == 0) *LDSP(volatile unsigned, lds0 + L_FLAG + lw * 4) = 0;
    auto rw = __builtin_amdgcn_make_buffer_rsrc((void*)w, 0, W_BYTES, 0x00020000);
    auto rx = __builtin_amdgcn_make_buffer_rsrc((void*)x, 0, X_BYTES, 0x00020000);
    auto rwo = __builtin_amdgcn_make_buffer_rsrc((void*)wo, 0, WO_BYTES, 0x00020000);
    if (producer) {
      const int wrow = lane >> 2;
      const unsigned wl = (g * 48 + wrow) * 576 + n * 64 + (((lane & 3) ^ ((wrow >> 3) << 1)) << 4);
      const unsigned o0 = ((lw >> 1) * 144 + (lw & 1) * 16) * 576;
      const unsigned o1 = o0 + 2 * 144 * 576;
      const unsigned o2 = (lw * 144 + 32) * 576;
      auto wblock = [&](int ord) {
        const unsigned tb = (((blk0 + ord) & 7) * 4 * 144 * 576);
        const unsigned dst = lds0 + L_W + ord * 12288 + lw * 1024;
        __builtin_amdgcn_raw_ptr_buffer_load_lds(rw, LDSP(void, dst), 16, wl + (tb + o0), 0, 0, 0);
        __builtin_amdgcn_raw_ptr_buffer_load_lds(rw, LDSP(void, dst + 4096), 16, wl + (tb + o1), 0, 0, 0);
        __builtin_amdgcn_raw_ptr_buffer_load_lds(rw, LDSP(void, dst + 8192), 16, wl + (tb + o2), 0, 0, 0);
      };
      wblock(0);
      const int xr = (lane * 43) >> 8;
      const int xc = lane - xr * 6;
      const int nx = (rA + 32 > 72) ? 29 : 26;
      const int baseA = ((sA >> 1) * 1000 + (sA & 1)) * 288 + g * 96 + xc * 16;
      const int sB = sA + 1;
      const int baseB = ((sB >> 1) * 1000 + (sB & 1)) * 288 + g * 96 + xc * 16;
      const int p0 = rA * 7 - 31 + xr;
      int k = (n % 3) * 9 + lw;
      if (lane < 60) {
        for (int kk = lw; kk < nx; kk += 4) {
          if (k >= nx) k -= nx;
          int p = p0 + k * 10;
          const bool second = p >= 504;
          p = second ? p - 536 : p;
          unsigned off = (second ? baseB : baseA) + p * 576;
          off = ((unsigned)p < 500u) ? off : 0x80000000u;
          __builtin_amdgcn_raw_ptr_buffer_load_lds(rx, LDSP(void, lds0 + L_X + k * 960), 16, off, 0, 0, 0);
          k += 4;
        }
      }
      asm volatile("s_waitcnt lgkmcnt(0)" ::: "memory");
      __builtin_amdgcn_s_barrier();
      STAMP(1);
      for (int ord = 1; ord < 8; ++ord) {
        wblock(ord);
        asm volatile("s_waitcnt vmcnt(3)" ::: "memory");
        if (lane == 0) *LDSP(volatile unsigned, lds0 + L_FLAG + lw * 4) = ord;
      }
      asm volatile("s_waitcnt vmcnt(0)" ::: "memory");
      if (lane == 0) *LDSP(volatile unsigned, lds0 + L_FLAG + lw * 4) = 8;
      STAMP(2);
      for (int sp = 0; sp < SPIN_MAX; ++sp) {
        u32x4 v = *LDSP(volatile u32x4, lds0 + L_FLAG + 16);
        if (__builtin_amdgcn_readfirstlane(v[0] & v[1] & v[2] & v[3]) == 1) break;
        __builtin_amdgcn_s_sleep(2);
      }
    } else {
      asm volatile("s_waitcnt lgkmcnt(0)" ::: "memory");
      __builtin_amdgcn_s_barrier();
    }
    for (int kk = lw; kk < 41; kk += 4)
      __builtin_amdgcn_raw_ptr_buffer_load_lds(rwo, LDSP(void, lds0 + L_W + kk * 1024), 16, kk * 1024 + lane * 16, 0, 0, 0);
    T = __hip_atomic_load((gu64*)(ws + WS_EPOCH + m * 128), RLX_AGENT) * 0x9E3779B97F4A7C15ull + 0x632BE59BD9B4E019ull;
    asm volatile("s_waitcnt vmcnt(0)" ::: "memory");
    STAMP(3);
  } else {
    if (lane == 0) *LDSP(volatile unsigned, lds0 + L_FLAG + 16 + wid * 4) = 0;
    T = __hip_atomic_load((gu64*)(ws + WS_EPOCH + m * 128), RLX_AGENT) * 0x9E3779B97F4A7C15ull + 0x632BE59BD9B4E019ull;
    asm volatile("s_waitcnt lgkmcnt(0)" ::: "memory");
    __builtin_amdgcn_s_barrier();
    if (producer) {
      const int mh = wid >> 1, nh = wid & 1;
      const int r = lane & 15, q = lane >> 4;
      const int rr = 16 * mh + r;
      const int lR = 31 + 7 * rr + ((rA + rr >= 72) ? 32 : 0);
      const unsigned xa0 = lds0 + L_X + (lR - 3) * 96 + 16 * q;
      const unsigned xb0 = lds0 + L_X + (lR - 2 - (q >> 1)) * 96 + 64 + 16 * (q & 1);
      unsigned wa = lds0 + L_W + (8 * q + (r >> 2)) * 64 + ((nh ^ (q & 1)) << 5) + (lane & 3) * 8;
      f32x4 acc[7];
#pragma unroll
      for (int j = 0; j < 7; ++j) acc[j] = f32x4{0.f, 0.f, 0.f, 0.f};
      auto wait_stage = [&](unsigned s) {
        for (int sp = 0; sp < SPIN_MAX; ++sp) {
          u32x4 v = *LDSP(volatile u32x4, lds0 + L_FLAG);
          unsigned mn = min(min(v[0], v[1]), min(v[2], v[3]));
          if (__builtin_amdgcn_readfirstlane(mn) >= s) break;
        }
      };
      wait_stage(1);
      STAMP(1);
      s16x8 xa[10], xb[9], wf0, wf1;
      int blk = blk0;
      unsigned xa_ = xa0 - blk * 384, xb_ = xb0 - blk * 384;
#pragma unroll
      for (int d = 3; d < 10; ++d) xa[d] = ld_frag(xa_ + d * 96);
      wf0 = tr_frag(wa);
      for (int ord = 0; ord < 8; ++ord) {
        const int nblk = (blk + 1) & 7;
        const unsigned nxa = xa0 - nblk * 384;
        wf1 = tr_frag(wa + 2048);
        xa[2] = ld_frag(xa_ + 2 * 96);
        xb[2] = ld_frag(xb_ + 2 * 96);
        xb[3] = ld_frag(xb_ + 3 * 96);
#pragma unroll
        for (int j = 0; j < 7; ++j) acc[j] = MFMA(wf0, xa[j + 3], acc[j]);
        __builtin_amdgcn_sched_barrier(0);
        wf0 = tr_frag(wa + 2 * 2048);
        xa[1] = ld_frag(xa_ + 1 * 96);
        xb[4] = ld_frag(xb_ + 4 * 96);
        xb[5] = ld_frag(xb_ + 5 * 96);
#pragma unroll
        for (int j = 0; j < 7; ++j) acc[j] = MFMA(wf1, xa[j + 2], acc[j]);
        __builtin_amdgcn_sched_barrier(0);
        wf1 = tr_frag(wa + 3 * 2048);
        xa[0] = ld_frag(xa_);
        xb[6] = ld_frag(xb_ + 6 * 96);
        xb[7] = ld_frag(xb_ + 7 * 96);
#pragma unroll
        for (int j = 0; j < 7; ++j) acc[j] = MFMA(wf0, xa[j + 1], acc[j]);
        __builtin_amdgcn_sched_barrier(0);
        wf0 = tr_frag(wa + 4 * 2048);
        xb[8] = ld_frag(xb_ + 8 * 96);
        xb[0] = ld_frag(xb_);
        xb[1] = ld_frag(xb_ + 96);
        xa[9] = ld_frag(nxa + 9 * 96);
#pragma unroll
        for (int j = 0; j < 7; ++j) acc[j] = MFMA(wf1, xa[j], acc[j]);
        __builtin_amdgcn_sched_barrier(0);
        wf1 = tr_frag(wa + 5 * 2048);
        xa[8] = ld_frag(nxa + 8 * 96);
        xa[7] = ld_frag(nxa + 7 * 96);
        xa[3] = ld_frag(nxa + 3 * 96);
#pragma unroll
        for (int j = 0; j < 7; ++j) acc[j] = MFMA(wf0, xb[j + 2], acc[j]);
        __builtin_amdgcn_sched_barrier(0);
        if (ord == 3) { asm volatile("s_waitcnt lgkmcnt(0)" ::: "memory"); if (lane == 0) *LDSP(volatile unsigned, lds0 + L_FLAG + 16 + wid * 4) = 1; }
        if (ord < 7) wait_stage(ord + 2);
        wa += 12288;
        wf0 = tr_frag(ord < 7 ? wa : wa - 12288);
        xa[4] = ld_frag(nxa + 4 * 96);
        xa[5] = ld_frag(nxa + 5 * 96);
        xa[6] = ld_frag(nxa + 6 * 96);
#pragma unroll
        for (int j = 0; j < 7; ++j) acc[j] = MFMA(wf1, xb[j], acc[j]);
        __builtin_amdgcn_sched_barrier(0);
        blk = nblk;
        xa_ = nxa;
        xb_ = xb0 - nblk * 384;
      }
      STAMP(2);
      auto rp = __builtin_amdgcn_make_buffer_rsrc((void*)(ws + WS_PART), 0, PART_BYTES, 0x00020000);
      unsigned pk[7][2];
#pragma unroll
      for (int j = 0; j < 7; ++j) { pk[j][0] = pk_bf16(acc[j][0], acc[j][1]); pk[j][1] = pk_bf16(acc[j][2], acc[j][3]); }
      const unsigned pbase = ((m * 28 + 2 * mh + (r >> 3)) * 27 + c) * 512 + nh * 256 + (r & 7) * 32;
#pragma unroll
      for (int jp = 0; jp < 3; ++jp) {
        auto s0 = __builtin_amdgcn_permlane16_swap(pk[2 * jp][0], pk[2 * jp + 1][0], false, false);
        auto s1 = __builtin_amdgcn_permlane16_swap(pk[2 * jp][1], pk[2 * jp + 1][1], false, false);
        u32x4 v = {s0[0], s1[0], s0[1], s1[1]};
        __builtin_amdgcn_raw_buffer_store_b128(v, rp, pbase + (2 * jp + (q & 1)) * (4 * 27 * 512) + (q >> 1) * 16, 0, 16);
      }
      {
        u32x2 v = {pk[6][0], pk[6][1]};
        __builtin_amdgcn_raw_buffer_store_b64(v, rp, pbase + 6 * (4 * 27 * 512) + q * 8, 0, 16);
      }
      asm volatile("s_waitcnt vmcnt(0)" ::: "memory");
      STAMP(3);
    }
  }
  asm volatile("s_waitcnt lgkmcnt(0)" ::: "memory");
  __builtin_amdgcn_s_barrier();
  if (producer && threadIdx.x == 0) __hip_atomic_store((gu64*)(ws + WS_FLAG + (m * 27 + c) * 128), T, RLX_AGENT);
  STAMP(4);
  const int q = lane >> 4;
  s16x8 of[5], of8[5];
  {
    const unsigned a0 = lds0 + L_W + (8 * q + ((lane & 15) >> 2)) * 288 + (lane & 3) * 8;
    const unsigned a4 = a0 + 128 * 288 - (q >> 1) * 16 * 288;
    auto trw = [&](unsigned a) {
      s16x4 lo = __builtin_amdgcn_ds_read_tr16_b64_v4i16(LDSP(s16x4, a));
      s16x4 hi = __builtin_amdgcn_ds_read_tr16_b64_v4i16(LDSP(s16x4, a + 4 * 288));
      return (s16x8)__builtin_shufflevector(lo, hi, 0, 1, 2, 3, 4, 5, 6, 7);
    };
#pragma unroll
    for (int s = 0; s < 4; ++s) { of[s] = trw(a0 + s * 32 * 288 + wid * 32); of8[s] = trw(a0 + s * 32 * 288 + 256); }
    of[4] = trw(a4 + wid * 32);
    of8[4] = trw(a4 + 256);
  }
  if (threadIdx.x >= 448 && threadIdx.x < 464) {
    const int t = threadIdx.x - 448;
    *LDSP(u32x4, lds0 + L_Z + (t >> 1) * 336 + 288 + (t & 1) * 16) = u32x4{0, 0, 0, 0};
  }
  if (wid < 5) {
    gu64* fl = (gu64*)(ws + WS_FLAG + (m * 27 + min(lane, 26)) * 128);
    for (int sp = 0; sp < SPIN_MAX; ++sp) {
      u64 v = __hip_atomic_load(fl, RLX_AGENT);
      if (__all(v == T)) break;
      __builtin_amdgcn_s_sleep(1);
    }
    STAMP(5);
    if (!producer && threadIdx.x == 0) __hip_atomic_store((gu64*)(ws + WS_EPOCH + m * 128), T, RLX_AGENT);
    asm volatile("" ::: "memory");
    if (threadIdx.x < 288) {
      auto rp = __builtin_amdgcn_make_buffer_rsrc((void*)(ws + WS_PART), 0, PART_BYTES, 0x00020000);
      const int t = threadIdx.x;
      const int rho = (t >> 2) & 7;
      const int cq = (t >> 5) * 4 + (t & 3);
      const unsigned cb = (m * 28 + c) * 27 * 512 + rho * 32 + (t & 3) * 8;
      const unsigned fo = cb + (t >> 5) * 256;
      const unsigned go = cb + (9 + (t >> 5)) * 256;
      u32x2 f0 = __builtin_amdgcn_raw_buffer_load_b64(rp, fo, 0, 16);
      u32x2 f1 = __builtin_amdgcn_raw_buffer_load_b64(rp, fo + 9 * 512, 0, 16);
      u32x2 f2 = __builtin_amdgcn_raw_buffer_load_b64(rp, fo + 18 * 512, 0, 16);
      u32x2 g0 = __builtin_amdgcn_raw_buffer_load_b64(rp, go, 0, 16);
      u32x2 g1 = __builtin_amdgcn_raw_buffer_load_b64(rp, go + 9 * 512, 0, 16);
      u32x2 g2 = __builtin_amdgcn_raw_buffer_load_b64(rp, go + 18 * 512, 0, 16);
      u32x2 z;
#pragma unroll
      for (int e = 0; e < 2; ++e) {
        float zl = gate(bf_lo(f0[e]) + bf_lo(f1[e]) + bf_lo(f2[e]), bf_lo(g0[e]) + bf_lo(g1[e]) + bf_lo(g2[e]));
        float zh = gate(bf_hi(f0[e]) + bf_hi(f1[e]) + bf_hi(f2[e]), bf_hi(g0[e]) + bf_hi(g1[e]) + bf_hi(g2[e]));
        z[e] = pk_bf16(zl, zh);
      }
      *LDSP(u32x2, lds0 + L_Z + rho * 336 + cq * 8) = z;
    }
    STAMP(6);
  }
  asm volatile("s_waitcnt lgkmcnt(0)" ::: "memory");
  __builtin_amdgcn_s_barrier();
  {
    const int rho = lane & 7;
    const unsigned za = lds0 + L_Z + rho * 336 + q * 16;
    s16x8 zf[5];
#pragma unroll
    for (int s = 0; s < 5; ++s) zf[s] = ld_frag(za + s * 64);
    f32x4 o = {0.f, 0.f, 0.f, 0.f}, o8 = {0.f, 0.f, 0.f, 0.f};
#pragma unroll
    for (int s = 0; s < 5; ++s) o = MFMA(of[s], zf[s], o);
    if (wid == 0) {
#pragma unroll
      for (int s = 0; s < 5; ++s) o8 = MFMA(of8[s], zf[s], o8);
    }
    int rr = rA + 8 * (c & 3) + rho;
    int sq = sA;
    if (rr >= 72) { rr -= 72; sq += 1; }
    const int p = rr * 7 + (c >> 2);
    if ((lane & 8) == 0 && p < 500) {
      char* orow = out + (size_t)(((sq >> 1) * 1000 + 2 * p + (sq & 1)) * 288 + q * 8);
      *(u32x2*)(orow + wid * 32) = u32x2{pk_bf16(o[0], o[1]), pk_bf16(o[2], o[3])};
      if (wid == 0) *(u32x2*)(orow + 256) = u32x2{pk_bf16(o8[0], o8[1]), pk_bf16(o8[2], o8[3])};
    }
  }
  STAMP(7);
}

extern "C" void kernel_launch(void* const* d_in, const int* in_sizes, int n_in, void* d_out, int out_size, void* d_ws,
                              size_t ws_size, hipStream_t stream) {
  wavenet_kernel<<<252, 512, 0, stream>>>((const char*)d_in[0], (const char*)d_in[1], (const char*)d_in[2], (char*)d_out,
                                          (char*)d_ws);
}
```

```cpp
#define TPROBE 0
#include <hip/hip_runtime.h>
#include <hip/hip_bf16.h>

typedef unsigned long long u64;
typedef __attribute__((ext_vector_type(8))) short s16x8;
typedef __attribute__((ext_vector_type(4))) short s16x4;
typedef __attribute__((ext_vector_type(4))) float f32x4;
typedef __attribute__((ext_vector_type(2))) float f32x2;
typedef __attribute__((ext_vector_type(4))) unsigned u32x4;
typedef __attribute__((ext_vector_type(2))) unsigned u32x2;
typedef __attribute__((ext_vector_type(2))) __bf16 bf16x2;
#define LDSP(T, a) ((__attribute__((address_space(3))) T*)(size_t)(a))

#define RLX_AGENT __ATOMIC_RELAXED, __HIP_MEMORY_SCOPE_AGENT
typedef __attribute__((address_space(1))) u64 gu64;

constexpr int W_BYTES = 31 * 144 * 288 * 2;
constexpr int X_BYTES = 2000 * 144 * 2;
constexpr int WO_BYTES = 144 * 144 * 2;
constexpr int L_W = 0;
constexpr int L_X = 98304;
constexpr int L_FLAG = L_X + 290 * 96;
constexpr int L_Z = L_FLAG + 64;
constexpr int L_END = L_Z + 8 * 336;
constexpr int WS_EPOCH = 0;
constexpr int WS_FLAG = 2048;
constexpr int WS_PART = 65536;
constexpr int PART_BYTES = 9 * 28 * 27 * 512;

constexpr int SPIN_MAX = 1 << 20;
#define STAMP(i) do { if ((i) == 0) { tp0 = tp1 = __builtin_amdgcn_s_memrealtime(); } else if ((i) == TPROBE) { tp1 = __builtin_amdgcn_s_memrealtime(); } \
   if ((i) == 7) { while (__builtin_amdgcn_s_memrealtime() - tp0 < 3000 + (tp1 - tp0)) __builtin_amdgcn_s_sleep(1); } } while (0)

__device__ __forceinline__ unsigned pk_bf16(float a, float b) {
  f32x2 v = {a, b};
  bf16x2 r = __builtin_convertvector(v, bf16x2);
  return __builtin_bit_cast(unsigned, r);
}
__device__ __forceinline__ float bf_lo(unsigned u) { return __builtin_bit_cast(float, u << 16); }
__device__ __forceinline__ float bf_hi(unsigned u) { return __builtin_bit_cast(float, u & 0xffff0000u); }

__device__ __forceinline__ s16x8 tr_frag(unsigned a) {
  s16x4 lo = __builtin_amdgcn_ds_read_tr16_b64_v4i16(LDSP(s16x4, a));
  s16x4 hi = __builtin_amdgcn_ds_read_tr16_b64_v4i16(LDSP(s16x4, a + 256));
  return __builtin_shufflevector(lo, hi, 0, 1, 2, 3, 4, 5, 6, 7);
}
__device__ __forceinline__ s16x8 ld_frag(unsigned a) { return *LDSP(s16x8, a); }
#define MFMA(a, b, c) __builtin_amdgcn_mfma_f32_16x16x32_bf16(a, b, c, 0, 0, 0)

__device__ __forceinline__ float gate(float f, float g) {
  f = __builtin_amdgcn_fmed3f(f, -20.f, 20.f);
  float E = __builtin_amdgcn_exp2f(f * 2.885390081777927f);
  float G = __builtin_amdgcn_exp2f(g * -1.4426950408889634f);
  return (E - 1.f) * __builtin_amdgcn_rcpf((E + 1.f) * (1.f + G));
}

__global__ __launch_bounds__(512, 2) void wavenet_kernel(const char* __restrict__ x, const char* __restrict__ w,
                                                         const char* __restrict__ wo, char* __restrict__ out,
                                                         char* __restrict__ ws
) {
  __shared__ __attribute__((aligned(1024))) char shm[L_END];
  const unsigned lds0 = (unsigned)(size_t)(__attribute__((address_space(3))) char*)shm;
  u64 tp0, tp1;
  STAMP(0);
  const int lane = threadIdx.x & 63;
  const int wid = __builtin_amdgcn_readfirstlane(threadIdx.x >> 6);
  const int b = blockIdx.x;
  const int xcd = b & 7;
  const int id = xcd * 31 + min(xcd, 4) + (b >> 3);
  const int c = id / 9, m = id - c * 9;
  const bool producer = c < 27;
  const int g = c / 9, n = c - g * 9;
  const int R0 = 32 * m;
  const int sA = R0 / 72;
  const int rA = R0 - sA * 72;
  const int blk0 = m & 7;
  u64 T;

  if (wid >= 4) {
    const int lw = wid - 4;
    if (lane == 0) *LDSP(volatile unsigned, lds0 + L_FLAG + lw * 4) = 0;
    auto rw = __builtin_amdgcn_make_buffer_rsrc((void*)w, 0, W_BYTES, 0x00020000);
    auto rx = __builtin_amdgcn_make_buffer_rsrc((void*)x, 0, X_BYTES, 0x00020000);
    auto rwo = __builtin_amdgcn_make_buffer_rsrc((void*)wo, 0, WO_BYTES, 0x00020000);
    asm volatile("s_waitcnt lgkmcnt(0)" :: "s"(w), "s"(x) : "memory");
    STAMP(8);
    if (producer) {
      const int wrow = lane >> 2;
      const unsigned wl = (g * 48 + wrow) * 576 + n * 64 + (((lane & 3) ^ ((wrow >> 3) << 1)) << 4);
      const unsigned o0 = ((lw >> 1) * 144 + (lw & 1) * 16) * 576;
      const unsigned o1 = o0 + 2 * 144 * 576;
      const unsigned o2 = (lw * 144 + 32) * 576;
      auto wblock = [&](int ord) {
        const unsigned tb = (((blk0 + ord) & 7) * 4 * 144 * 576);
        const unsigned dst = lds0 + L_W + ord * 12288 + lw * 1024;
        __builtin_amdgcn_raw_ptr_buffer_load_lds(rw, LDSP(void, dst), 16, wl + (tb + o0), 0, 0, 0);
        __builtin_amdgcn_raw_ptr_buffer_load_lds(rw, LDSP(void, dst + 4096), 16, wl + (tb + o1), 0, 0, 0);
        __builtin_amdgcn_raw_ptr_buffer_load_lds(rw, LDSP(void, dst + 8192), 16, wl + (tb + o2), 0, 0, 0);
      };
      STAMP(9);
      wblock(0);
      const int xr = (lane * 43) >> 8;
      const int xc = lane - xr * 6;
      const int nx = (rA + 32 > 72) ? 29 : 26;
      const int baseA = ((sA >> 1) * 1000 + (sA & 1)) * 288 + g * 96 + xc * 16;
      const int sB = sA + 1;
      const int baseB = ((sB >> 1) * 1000 + (sB & 1)) * 288 + g * 96 + xc * 16;
      const int p0 = rA * 7 - 31 + xr;
      int k = (n % 3) * 9 + lw;
      if (lane < 60) {
        for (int kk = lw; kk < nx; kk += 4) {
          if (k >= nx) k -= nx;
          int p = p0 + k * 10;
          const bool second = p >= 504;
          p = second ? p - 536 : p;
          unsigned off = (second ? baseB : baseA) + p * 576;
          off = ((unsigned)p < 500u) ? off : 0x80000000u;
          __builtin_amdgcn_raw_ptr_buffer_load_lds(rx, LDSP(void, lds0 + L_X + k * 960), 16, off, 0, 0, 0);
          k += 4;
        }
      }
      STAMP(11);
      asm volatile("s_waitcnt lgkmcnt(0)" ::: "memory");
      __builtin_amdgcn_s_barrier();
      for (int ord = 1; ord < 8; ++ord) {
        wblock(ord);
        asm volatile("s_waitcnt vmcnt(3)" ::: "memory");
        if (lane == 0) *LDSP(volatile unsigned, lds0 + L_FLAG + lw * 4) = ord;
      }
      asm volatile("s_waitcnt vmcnt(0)" ::: "memory");
      if (lane == 0) *LDSP(volatile unsigned, lds0 + L_FLAG + lw * 4) = 8;
      STAMP(12);
      for (int sp = 0; sp < SPIN_MAX; ++sp) {
        u32x4 v = *LDSP(volatile u32x4, lds0 + L_FLAG + 16);
        if (__builtin_amdgcn_readfirstlane(v[0] & v[1] & v[2] & v[3]) == 1) break;
        __builtin_amdgcn_s_sleep(2);
      }
    } else {
      asm volatile("s_waitcnt lgkmcnt(0)" ::: "memory");
      __builtin_amdgcn_s_barrier();
    }
    for (int kk = lw; kk < 41; kk += 4)
      __builtin_amdgcn_raw_ptr_buffer_load_lds(rwo, LDSP(void, lds0 + L_W + kk * 1024), 16, kk * 1024 + lane * 16, 0, 0, 0);
    T = __hip_atomic_load((gu64*)(ws + WS_EPOCH + m * 128), RLX_AGENT) * 0x9E3779B97F4A7C15ull + 0x632BE59BD9B4E019ull;
    asm volatile("s_waitcnt vmcnt(0)" ::: "memory");
    STAMP(3);
  } else {
    if (lane == 0) *LDSP(volatile unsigned, lds0 + L_FLAG + 16 + wid * 4) = 0;
    T = __hip_atomic_load((gu64*)(ws + WS_EPOCH + m * 128), RLX_AGENT) * 0x9E3779B97F4A7C15ull + 0x632BE59BD9B4E019ull;
    asm volatile("s_waitcnt lgkmcnt(0)" ::: "memory");
    __builtin_amdgcn_s_barrier();
    if (producer) {
      const int mh = wid >> 1, nh = wid & 1;
      const int r = lane & 15, q = lane >> 4;
      const int rr = 16 * mh + r;
      const int lR = 31 + 7 * rr + ((rA + rr >= 72) ? 32 : 0);
      const unsigned xa0 = lds0 + L_X + (lR - 3) * 96 + 16 * q;
      const unsigned xb0 = lds0 + L_X + (lR - 2 - (q >> 1)) * 96 + 64 + 16 * (q & 1);
      unsigned wa = lds0 + L_W + (8 * q + (r >> 2)) * 64 + ((nh ^ (q & 1)) << 5) + (lane & 3) * 8;
      f32x4 acc[7];
#pragma unroll
      for (int j = 0; j < 7; ++j) acc[j] = f32x4{0.f, 0.f, 0.f, 0.f};
      auto wait_stage = [&](unsigned s) {
        for (int sp = 0; sp < SPIN_MAX; ++sp) {
          u32x4 v = *LDSP(volatile u32x4, lds0 + L_FLAG);
          unsigned mn = min(min(v[0], v[1]), min(v[2], v[3]));
          if (__builtin_amdgcn_readfirstlane(mn) >= s) break;
        }
      };
      wait_stage(1);
      STAMP(1);
      s16x8 xa[10], xb[9], wf0, wf1;
      int blk = blk0;
      unsigned xa_ = xa0 - blk * 384, xb_ = xb0 - blk * 384;
#pragma unroll
      for (int d = 3; d < 10; ++d) xa[d] = ld_frag(xa_ + d * 96);
      wf0 = tr_frag(wa);
      for (int ord = 0; ord < 8; ++ord) {
        const int nblk = (blk + 1) & 7;
        const unsigned nxa = xa0 - nblk * 384;
        wf1 = tr_frag(wa + 2048);
        xa[2] = ld_frag(xa_ + 2 * 96);
        xb[2] = ld_frag(xb_ + 2 * 96);
        xb[3] = ld_frag(xb_ + 3 * 96);
#pragma unroll
        for (int j = 0; j < 7; ++j) acc[j] = MFMA(wf0, xa[j + 3], acc[j]);
        __builtin_amdgcn_sched_barrier(0);
        wf0 = tr_frag(wa + 2 * 2048);
        xa[1] = ld_frag(xa_ + 1 * 96);
        xb[4] = ld_frag(xb_ + 4 * 96);
        xb[5] = ld_frag(xb_ + 5 * 96);
#pragma unroll
        for (int j = 0; j < 7; ++j) acc[j] = MFMA(wf1, xa[j + 2], acc[j]);
        __builtin_amdgcn_sched_barrier(0);
        wf1 = tr_frag(wa + 3 * 2048);
        xa[0] = ld_frag(xa_);
        xb[6] = ld_frag(xb_ + 6 * 96);
        xb[7] = ld_frag(xb_ + 7 * 96);
#pragma unroll
        for (int j = 0; j < 7; ++j) acc[j] = MFMA(wf0, xa[j + 1], acc[j]);
        __builtin_amdgcn_sched_barrier(0);
        wf0 = tr_frag(wa + 4 * 2048);
        xb[8] = ld_frag(xb_ + 8 * 96);
        xb[0] = ld_frag(xb_);
        xb[1] = ld_frag(xb_ + 96);
        xa[9] = ld_frag(nxa + 9 * 96);
#pragma unroll
        for (int j = 0; j < 7; ++j) acc[j] = MFMA(wf1, xa[j], acc[j]);
        __builtin_amdgcn_sched_barrier(0);
        wf1 = tr_frag(wa + 5 * 2048);
        xa[8] = ld_frag(nxa + 8 * 96);
        xa[7] = ld_frag(nxa + 7 * 96);
        xa[3] = ld_frag(nxa + 3 * 96);
#pragma unroll
        for (int j = 0; j < 7; ++j) acc[j] = MFMA(wf0, xb[j + 2], acc[j]);
        __builtin_amdgcn_sched_barrier(0);
        if (ord == 3) { asm volatile("s_waitcnt lgkmcnt(0)" ::: "memory"); if (lane == 0) *LDSP(volatile unsigned, lds0 + L_FLAG + 16 + wid * 4) = 1; }
        if (ord < 7) wait_stage(ord + 2);
        wa += 12288;
        wf0 = tr_frag(ord < 7 ? wa : wa - 12288);
        xa[4] = ld_frag(nxa + 4 * 96);
        xa[5] = ld_frag(nxa + 5 * 96);
        xa[6] = ld_frag(nxa + 6 * 96);
#pragma unroll
        for (int j = 0; j < 7; ++j) acc[j] = MFMA(wf1, xb[j], acc[j]);
        __builtin_amdgcn_sched_barrier(0);
        blk = nblk;
        xa_ = nxa;
        xb_ = xb0 - nblk * 384;
      }
      STAMP(2);
      auto rp = __builtin_amdgcn_make_buffer_rsrc((void*)(ws + WS_PART), 0, PART_BYTES, 0x00020000);
      unsigned pk[7][2];
#pragma unroll
      for (int j = 0; j < 7; ++j) { pk[j][0] = pk_bf16(acc[j][0], acc[j][1]); pk[j][1] = pk_bf16(acc[j][2], acc[j][3]); }
      const unsigned pbase = ((m * 28 + 2 * mh + (r >> 3)) * 27 + c) * 512 + nh * 256 + (r & 7) * 32;
#pragma unroll
      for (int jp = 0; jp < 3; ++jp) {
        auto s0 = __builtin_amdgcn_permlane16_swap(pk[2 * jp][0], pk[2 * jp + 1][0], false, false);
        auto s1 = __builtin_amdgcn_permlane16_swap(pk[2 * jp][1], pk[2 * jp + 1][1], false, false);
        u32x4 v = {s0[0], s1[0], s0[1], s1[1]};
        __builtin_amdgcn_raw_buffer_store_b128(v, rp, pbase + (2 * jp + (q & 1)) * (4 * 27 * 512) + (q >> 1) * 16, 0, 16);
      }
      {
        u32x2 v = {pk[6][0], pk[6][1]};
        __builtin_amdgcn_raw_buffer_store_b64(v, rp, pbase + 6 * (4 * 27 * 512) + q * 8, 0, 16);
      }
      asm volatile("s_waitcnt vmcnt(0)" ::: "memory");
      STAMP(3);
    }
  }
  asm volatile("s_waitcnt lgkmcnt(0)" ::: "memory");
  __builtin_amdgcn_s_barrier();
  if (producer && threadIdx.x == 0) __hip_atomic_store((gu64*)(ws + WS_FLAG + (m * 27 + c) * 128), T, RLX_AGENT);
  STAMP(4);
  const int q = lane >> 4;
  s16x8 of[5], of8[5];
  {
    const unsigned a0 = lds0 + L_W + (8 * q + ((lane & 15) >> 2)) * 288 + (lane & 3) * 8;
    const unsigned a4 = a0 + 128 * 288 - (q >> 1) * 16 * 288;
    auto trw = [&](unsigned a) {
      s16x4 lo = __builtin_amdgcn_ds_read_tr16_b64_v4i16(LDSP(s16x4, a));
      s16x4 hi = __builtin_amdgcn_ds_read_tr16_b64_v4i16(LDSP(s16x4, a + 4 * 288));
      return (s16x8)__builtin_shufflevector(lo, hi, 0, 1, 2, 3, 4, 5, 6, 7);
    };
#pragma unroll
    for (int s = 0; s < 4; ++s) { of[s] = trw(a0 + s * 32 * 288 + wid * 32); of8[s] = trw(a0 + s * 32 * 288 + 256); }
    of[4] = trw(a4 + wid * 32);
    of8[4] = trw(a4 + 256);
  }
  if (threadIdx.x >= 448 && threadIdx.x < 464) {
    const int t = threadIdx.x - 448;
    *LDSP(u32x4, lds0 + L_Z + (t >> 1) * 336 + 288 + (t & 1) * 16) = u32x4{0, 0, 0, 0};
  }
  if (wid < 5) {
    gu64* fl = (gu64*)(ws + WS_FLAG + (m * 27 + min(lane, 26)) * 128);
    for (int sp = 0; sp < SPIN_MAX; ++sp) {
      u64 v = __hip_atomic_load(fl, RLX_AGENT);
      if (__all(v == T)) break;
      __builtin_amdgcn_s_sleep(1);
    }
    STAMP(5);
    if (!producer && threadIdx.x == 0) __hip_atomic_store((gu64*)(ws + WS_EPOCH + m * 128), T, RLX_AGENT);
    asm volatile("" ::: "memory");
    if (threadIdx.x < 288) {
      auto rp = __builtin_amdgcn_make_buffer_rsrc((void*)(ws + WS_PART), 0, PART_BYTES, 0x00020000);
      const int t = threadIdx.x;
      const int rho = (t >> 2) & 7;
      const int cq = (t >> 5) * 4 + (t & 3);
      const unsigned cb = (m * 28 + c) * 27 * 512 + rho * 32 + (t & 3) * 8;
      const unsigned fo = cb + (t >> 5) * 256;
      const unsigned go = cb + (9 + (t >> 5)) * 256;
      u32x2 f0 = __builtin_amdgcn_raw_buffer_load_b64(rp, fo, 0, 16);
      u32x2 f1 = __builtin_amdgcn_raw_buffer_load_b64(rp, fo + 9 * 512, 0, 16);
      u32x2 f2 = __builtin_amdgcn_raw_buffer_load_b64(rp, fo + 18 * 512, 0, 16);
      u32x2 g0 = __builtin_amdgcn_raw_buffer_load_b64(rp, go, 0, 16);
      u32x2 g1 = __builtin_amdgcn_raw_buffer_load_b64(rp, go + 9 * 512, 0, 16);
      u32x2 g2 = __builtin_amdgcn_raw_buffer_load_b64(rp, go + 18 * 512, 0, 16);
      u32x2 z;
#pragma unroll
      for (int e = 0; e < 2; ++e) {
        float zl = gate(bf_lo(f0[e]) + bf_lo(f1[e]) + bf_lo(f2[e]), bf_lo(g0[e]) + bf_lo(g1[e]) + bf_lo(g2[e]));
        float zh = gate(bf_hi(f0[e]) + bf_hi(f1[e]) + bf_hi(f2[e]), bf_hi(g0[e]) + bf_hi(g1[e]) + bf_hi(g2[e]));
        z[e] = pk_bf16(zl, zh);
      }
      *LDSP(u32x2, lds0 + L_Z + rho * 336 + cq * 8) = z;
    }
    STAMP(6);
  }
  asm volatile("s_waitcnt lgkmcnt(0)" ::: "memory");
  __builtin_amdgcn_s_barrier();
  {
    const int rho = lane & 7;
    const unsigned za = lds0 + L_Z + rho * 336 + q * 16;
    s16x8 zf[5];
#pragma unroll
    for (int s = 0; s < 5; ++s) zf[s] = ld_frag(za + s * 64);
    f32x4 o = {0.f, 0.f, 0.f, 0.f}, o8 = {0.f, 0.f, 0.f, 0.f};
#pragma unroll
    for (int s = 0; s < 5; ++s) o = MFMA(of[s], zf[s], o);
    if (wid == 0) {
#pragma unroll
      for (int s = 0; s < 5; ++s) o8 = MFMA(of8[s], zf[s], o8);
    }
    int rr = rA + 8 * (c & 3) + rho;
    int sq = sA;
    if (rr >= 72) { rr -= 72; sq += 1; }
    const int p = rr * 7 + (c >> 2);
    if ((lane & 8) == 0 && p < 500) {
      char* orow = out + (size_t)(((sq >> 1) * 1000 + 2 * p + (sq & 1)) * 288 + q * 8);
      *(u32x2*)(orow + wid * 32) = u32x2{pk_bf16(o[0], o[1]), pk_bf16(o[2], o[3])};
      if (wid == 0) *(u32x2*)(orow + 256) = u32x2{pk_bf16(o8[0], o8[1]), pk_bf16(o8[2], o8[3])};
    }
  }
  STAMP(7);
}

extern "C" void kernel_launch(void* const* d_in, const int* in_sizes, int n_in, void* d_out, int out_size, void* d_ws,
                              size_t ws_size, hipStream_t stream) {
  wavenet_kernel<<<252, 512, 0, stream>>>((const char*)d_in[0], (const char*)d_in[1], (const char*)d_in[2], (char*)d_out,
                                          (char*)d_ws);
}
```

```cpp
#define SWEEP_DELAY 5
#include <hip/hip_runtime.h>
#include <hip/hip_bf16.h>

typedef unsigned long long u64;
typedef __attribute__((ext_vector_type(8))) short s16x8;
typedef __attribute__((ext_vector_type(4))) short s16x4;
typedef __attribute__((ext_vector_type(4))) float f32x4;
typedef __attribute__((ext_vector_type(2))) float f32x2;
typedef __attribute__((ext_vector_type(4))) unsigned u32x4;
typedef __attribute__((ext_vector_type(8))) unsigned u32x8;
typedef __attribute__((ext_vector_type(2))) unsigned u32x2;
typedef __attribute__((ext_vector_type(2))) __bf16 bf16x2;
#define LDSP(T, a) ((__attribute__((address_space(3))) T*)(size_t)(a))

#define RLX_AGENT __ATOMIC_RELAXED, __HIP_MEMORY_SCOPE_AGENT
typedef __attribute__((address_space(1))) u64 gu64;

constexpr int W_BYTES = 31 * 144 * 288 * 2;
constexpr int X_BYTES = 2000 * 144 * 2;
constexpr int WO_BYTES = 144 * 144 * 2;
constexpr int L_W = 0;
constexpr int L_X = 98304;
constexpr int L_FLAG = L_X + 290 * 96;
constexpr int L_PROG = L_FLAG + 64;
constexpr int L_Z = L_PROG + 1024;
constexpr int L_PF = L_Z + 8 * 336;
constexpr int L_END = L_PF + 256;
#define PF_LINES 45
#define CLR_AFTER 2
#define CLR_SLEEP 0
#define POLL_SLEEP 6
#define RETRY_MASK 1
#define RETRY_SLEEP 1
constexpr int WS_EPOCH = 0;
constexpr int WS_FLAG = 2048;
constexpr int WS_PART = 65536;
constexpr int PART_BYTES = 9 * 28 * 27 * 512;
constexpr int SPIN_MAX = 1 << 20;

#define STAMP(i)

__device__ __forceinline__ unsigned pk_bf16(float a, float b) {
  f32x2 v = {a, b};
  bf16x2 r = __builtin_convertvector(v, bf16x2);
  return __builtin_bit_cast(unsigned, r);
}
__device__ __forceinline__ float bf_lo(unsigned u) { return __builtin_bit_cast(float, u << 16); }
__device__ __forceinline__ float bf_hi(unsigned u) { return __builtin_bit_cast(float, u & 0xffff0000u); }
__device__ __forceinline__ s16x8 ld_frag(unsigned a) { return *LDSP(s16x8, a); }
#define MFMA(a, b, c) __builtin_amdgcn_mfma_f32_16x16x32_bf16(a, b, c, 0, 0, 0)

__device__ __forceinline__ float gate(float f, float g) {
  float E = __builtin_amdgcn_exp2f(fminf(f * 2.885390081777927f, 60.f));
  float G = __builtin_amdgcn_exp2f(g * -1.4426950408889634f);
  float A = E + 1.f;
  return (E - 1.f) * __builtin_amdgcn_rcpf(__builtin_fmaf(A, G, A));
}
__device__ __forceinline__ const char* mkptr(unsigned lo, unsigned hi) { return (const char*)(((u64)hi << 32) | lo); }

#define KWAIT "s_waitcnt lgkmcnt(0)"

__global__ __launch_bounds__(576, 3) void wavenet_kernel(const char* __restrict__ x_, const char* __restrict__ w_,
                                                         const char* __restrict__ wo_, char* __restrict__ out_,
                                                         char* __restrict__ ws_
) {
  __shared__ __attribute__((aligned(1024))) char shm[L_END];
  const unsigned lds0 = (unsigned)(size_t)(__attribute__((address_space(3))) char*)shm;
  u32x8 ka;
  u32x2 kb;
  const char* pc0;
  {
    auto kp = __builtin_amdgcn_kernarg_segment_ptr();
    asm volatile("s_load_dwordx8 s[88:95], %1, 0x0\n\ts_load_dwordx2 s[96:97], %1, 0x20\n\ts_getpc_b64 %0" : "=&s"(pc0) : "s"(kp)
                 : "s88", "s89", "s90", "s91", "s92", "s93", "s94", "s95", "s96", "s97");
  }
  if (threadIdx.x < PF_LINES) {
    asm volatile("s_mov_b32 m0, %0\n\ts_nop 0\n\tglobal_load_lds_dword %1, %2" :: "s"(lds0 + L_PF), "v"(threadIdx.x * 128), "s"(pc0) : "m0");
  }
  STAMP(0);
  const int lane = threadIdx.x & 63;
  const int wid = __builtin_amdgcn_readfirstlane(threadIdx.x >> 6);
  const unsigned b = blockIdx.x;
  const unsigned xcd = b & 7;
  const unsigned id = xcd * 31 + min(xcd, 4u) + (b >> 3);
  const int c = (id * 57) >> 9, m = id - c * 9;
  const bool producer = c < 27;
  const int g = (c * 57) >> 9, n = c - g * 9;
  const int sA = (m * 57) >> 7;
  const int rA = 32 * m - sA * 72;
    char* ws;
  if (__builtin_expect(wid & 4, 1)) {
    const int lw = wid - 4;
    *LDSP(volatile unsigned char, lds0 + L_FLAG + lw) = 0;
    const int wrow = lane >> 2;
    const unsigned wl = (g * 48 + wrow) * 576 + n * 64 + (((lane & 3) ^ ((wrow >> 3) << 1)) << 4);
    unsigned w0 = wl + (lw == 2 ? 32 : lw * 288) * 576;
    int wd1 = (lw == 2 ? 144 : 16) * 576, wd2 = (lw == 2 ? 288 : 144) * 576;
    const int xr = (lane * 43) >> 8;
    const int xc = lane - xr * 6;
    int nxu = (rA + 32 > 72) ? 29 * 960 : 26 * 960;
    int baseA = ((sA >> 1) * 1000 + (sA & 1)) * 288 + g * 96;
    int dB = ((sA & 1) ? 999 * 288 : 288) - 536 * 576;
    int y0 = (rA * 7 - 31 + xr) * 576 + xc * 16;
    int xu = ((n >= 6 ? n - 6 : n >= 3 ? n - 3 : n) * 9 + lw) * 960;
    unsigned tb = 0;
    asm volatile(KWAIT
                 : "={s[88:95]}"(ka), "={s[96:97]}"(kb), "+v"(w0), "+s"(wd1), "+s"(wd2), "+v"(y0), "+s"(nxu), "+s"(baseA), "+s"(dB), "+s"(xu));
    STAMP(8);
    ws = (char*)mkptr(kb[0], kb[1]);
    auto rw = __builtin_amdgcn_make_buffer_rsrc((void*)mkptr(ka[2], ka[3]), 0, W_BYTES, 0x00020000);
    auto rx = __builtin_amdgcn_make_buffer_rsrc((void*)mkptr(ka[0], ka[1]), 0, X_BYTES, 0x00020000);
    auto rwo = __builtin_amdgcn_make_buffer_rsrc((void*)mkptr(ka[4], ka[5]), 0, WO_BYTES, 0x00020000);
    auto rp = __builtin_amdgcn_make_buffer_rsrc((void*)(ws + WS_PART), 0, PART_BYTES, 0x00020000);
    u64 E;
    if (__builtin_expect(producer, 1)) {
      unsigned dst = lds0 + L_W + lw * 4096;
      auto wblock = [&]() {
        __builtin_amdgcn_raw_ptr_buffer_load_lds(rw, LDSP(void, dst), 16, w0, tb, 0, 0);
        __builtin_amdgcn_raw_ptr_buffer_load_lds(rw, LDSP(void, dst + 1024), 16, w0, tb + wd1, 0, 0);
        __builtin_amdgcn_raw_ptr_buffer_load_lds(rw, LDSP(void, dst + 2048), 16, w0 + (tb + wd2), 0, 0, 0);
        __builtin_amdgcn_raw_ptr_buffer_load_lds(rw, LDSP(void, dst + 3072), 16, w0 + (tb + wd2 + wd1), 0, 0, 0);
        dst += 12288;
        tb += 4 * 144 * 576;
      };
      if (lw < 3) wblock();
      if (lane < 60) {
#pragma unroll 1
        for (int kk = lw * 960; kk < nxu; kk += 3840) {
          const int y = y0 + xu * 6;
          const unsigned off = y >= 536 * 576 ? y + dB : ((unsigned)y < 500u * 576u ? y : 0x80000000u);
          __builtin_amdgcn_raw_ptr_buffer_load_lds(rx, LDSP(void, lds0 + L_X + xu), 16, off, baseA, 0, 0);
          xu += 3840;
          if (xu >= nxu) xu -= nxu;
        }
      }
      STAMP(11);
      __builtin_amdgcn_s_barrier();
      if (lw < 3) {
        unsigned done = 1;
#pragma unroll 1
        for (int ord = 1; ord < 8; ++ord) {
          wblock();
          asm volatile("s_waitcnt vmcnt(4)" ::: "memory");
          *LDSP(volatile unsigned char, lds0 + L_FLAG + lw) = done;
          done = done * 2 + 1;
        }
      } else {
        asm volatile("s_waitcnt vmcnt(0)" ::: "memory");
        *LDSP(volatile unsigned char, lds0 + L_FLAG + lw) = 127;
        E = __hip_atomic_load((gu64*)(ws + WS_EPOCH + m * 128), RLX_AGENT);
        unsigned o = ((m * 28 + (lane >> 5)) * 27 + c) * 512 + (lane & 31) * 16;
#pragma unroll 1
        for (int sp = 0; sp < SPIN_MAX; ++sp) {
          __builtin_amdgcn_s_sleep(4);
          unsigned v = *LDSP(volatile unsigned, lds0 + L_FLAG);
          if (__builtin_amdgcn_readfirstlane(v) & CLR_AFTER) break;
        }
#pragma unroll 1
        for (int i = 0; i < 14; ++i, o += 2 * 27 * 512) {
          __builtin_amdgcn_raw_buffer_store_b128(u32x4{~0u, ~0u, ~0u, ~0u}, rp, o, 0, 16);
          if (CLR_SLEEP) __builtin_amdgcn_s_sleep(CLR_SLEEP);
        }
      }
      asm volatile("s_waitcnt vmcnt(0)" ::: "memory");
      *LDSP(volatile unsigned char, lds0 + L_FLAG + lw) = 255;
      STAMP(12);
    } else {
      __builtin_amdgcn_s_barrier();
    }
    if (lw == 3) {
      if (!producer) E = __hip_atomic_load((gu64*)(ws + WS_EPOCH + m * 128), RLX_AGENT);
      const u64 T = E * 0x9E3779B97F4A7C15ull + 0x632BE59BD9B4E019ull;
      if (producer && lane == 0) __hip_atomic_store((gu64*)(ws + WS_FLAG + (m * 27 + c) * 128), T, RLX_AGENT);
      gu64* fl = (gu64*)(ws + WS_FLAG + (m * 27 + min(lane, 26)) * 128);
#pragma unroll 1
      for (int sp = 0; sp < SPIN_MAX; ++sp) {
        __builtin_amdgcn_s_sleep(POLL_SLEEP);
        u64 v = __hip_atomic_load(fl, RLX_AGENT);
        if (__all(v == T)) break;
      }
      if (!producer && lane == 0) __hip_atomic_store((gu64*)(ws + WS_EPOCH + m * 128), T, RLX_AGENT);
      if (lane < 16) *LDSP(u32x4, lds0 + L_Z + (lane >> 1) * 336 + 288 + (lane & 1) * 16) = u32x4{0, 0, 0, 0};
    } else {
      if (producer) {
#pragma unroll 1
        for (int sp = 0; sp < SPIN_MAX; ++sp) {
          unsigned v = *LDSP(volatile unsigned, lds0 + L_PROG + (lane & 3) * 256);
          if (__all(v >= lds0 + 4 * 12288)) break;
          __builtin_amdgcn_s_sleep(4);
        }
      }
#pragma unroll 1
      for (int kk = lw; kk < 41; kk += 3)
        __builtin_amdgcn_raw_ptr_buffer_load_lds(rwo, LDSP(void, lds0 + L_W + kk * 1024), 16, lane * 16 + kk * 1024, 0, 0, 0);
    }
    __builtin_amdgcn_s_waitcnt(0x0F70);
    STAMP(3);
  } else {
    const unsigned vpa = lds0 + L_PROG + (wid & 3) * 256 + lane * 4;
    *LDSP(volatile unsigned, vpa) = 0;
    asm volatile(KWAIT "\n\ts_barrier" : "={s[88:95]}"(ka), "={s[96:97]}"(kb));
    ws = (char*)mkptr(kb[0], kb[1]);
    if (producer && wid < 4) {
      const int mh = wid >> 1, nh = wid & 1;
      const int r = lane & 15, q = lane >> 4;
      const int rr = 16 * mh + r;
      const int lR = 31 + 7 * rr + ((rA + rr >= 72) ? 32 : 0);
      unsigned vxa = lds0 + L_X + (lR - 5) * 96 + 16 * q;
      unsigned vxb = lds0 + L_X + (lR - (q >> 1) - 5) * 96 + 64 + 16 * (q & 1);
      unsigned vw = lds0 + L_W + (8 * q + (r >> 2)) * 64 + ((nh ^ (q & 1)) << 5) + (lane & 3) * 8;
      f32x4 a0 = {0.f, 0.f, 0.f, 0.f}, a1 = a0, a2 = a0, a3 = a0, a4 = a0, a5 = a0, a6 = a0;
      unsigned snd = 0x02020202u, st, sc, vfl;
      const unsigned vfa = lds0 + L_FLAG;
      unsigned pb1 = ((m * 28 + (q & 1) * 4 + 2 * mh + (r >> 3)) * 27 + c) * 512 + nh * 256 + (q >> 1) * 128 + (r & 7) * 16;
      unsigned pb2 = (q & 1) ? 0x80000000u : pb1;
#pragma unroll 1
      for (int sp = 0; sp < SPIN_MAX; ++sp) {
        unsigned v = *LDSP(volatile unsigned, vfa);
        if ((__builtin_amdgcn_readfirstlane(v) & 0x01010101u) == 0x01010101u) break;
      }
      asm volatile("s_waitcnt vmcnt(0)");
      STAMP(1);
      asm volatile(
        "s_setprio 3\n"
        "ds_read_b64_tr_b16 v[104:105], %[vw] offset:0\n"
        "ds_read_b64_tr_b16 v[106:107], %[vw] offset:256\n"
        "ds_read_b128 v[64:67], %[vxa] offset:1056\n"
        "ds_read_b128 v[60:63], %[vxa] offset:960\n"
        "ds_read_b128 v[56:59], %[vxa] offset:864\n"
        "ds_read_b128 v[52:55], %[vxa] offset:768\n"
        "ds_read_b128 v[48:51], %[vxa] offset:672\n"
        "ds_read_b128 v[44:47], %[vxa] offset:576\n"
        "ds_read_b128 v[40:43], %[vxa] offset:480\n"
        "ds_read_b64_tr_b16 v[108:109], %[vw] offset:2048\n"
        "ds_read_b64_tr_b16 v[110:111], %[vw] offset:2304\n"
        "ds_read_b128 v[68:71], %[vxa] offset:384\n"
        "s_waitcnt lgkmcnt(9)\n"
        "v_mfma_f32_16x16x32_bf16 %[a6], v[104:107], v[64:67], %[a6]\n"
        "ds_read_b64_tr_b16 v[112:113], %[vw] offset:8192\n"
        "s_waitcnt lgkmcnt(9)\n"
        "v_mfma_f32_16x16x32_bf16 %[a5], v[104:107], v[60:63], %[a5]\n"
        "ds_read_b64_tr_b16 v[114:115], %[vw] offset:8448\n"
        "s_waitcnt lgkmcnt(9)\n"
        "v_mfma_f32_16x16x32_bf16 %[a4], v[104:107], v[56:59], %[a4]\n"
        "ds_read_b128 v[96:99], %[vxb] offset:1056\n"
        "s_waitcnt lgkmcnt(9)\n"
        "v_mfma_f32_16x16x32_bf16 %[a3], v[104:107], v[52:55], %[a3]\n"
        "ds_read_b128 v[92:95], %[vxb] offset:960\n"
        "s_waitcnt lgkmcnt(9)\n"
        "v_mfma_f32_16x16x32_bf16 %[a2], v[104:107], v[48:51], %[a2]\n"
        "ds_read_b128 v[88:91], %[vxb] offset:864\n"
        "s_waitcnt lgkmcnt(9)\n"
        "v_mfma_f32_16x16x32_bf16 %[a1], v[104:107], v[44:47], %[a1]\n"
        "ds_read_b128 v[84:87], %[vxb] offset:768\n"
        "s_waitcnt lgkmcnt(9)\n"
        "v_mfma_f32_16x16x32_bf16 %[a0], v[104:107], v[40:43], %[a0]\n"
        "ds_read_b128 v[80:83], %[vxb] offset:672\n"
        "s_waitcnt lgkmcnt(8)\n"
        "v_mfma_f32_16x16x32_bf16 %[a6], v[108:111], v[60:63], %[a6]\n"
        "ds_read_b128 v[76:79], %[vxb] offset:576\n"
        "v_mfma_f32_16x16x32_bf16 %[a5], v[108:111], v[56:59], %[a5]\n"
        "ds_read_b128 v[72:75], %[vxb] offset:480\n"
        "v_mfma_f32_16x16x32_bf16 %[a4], v[108:111], v[52:55], %[a4]\n"
        "ds_read_b64_tr_b16 v[104:105], %[vw] offset:4096\n"
        "v_mfma_f32_16x16x32_bf16 %[a3], v[108:111], v[48:51], %[a3]\n"
        "ds_read_b64_tr_b16 v[106:107], %[vw] offset:4352\n"
        "v_mfma_f32_16x16x32_bf16 %[a2], v[108:111], v[44:47], %[a2]\n"
        "ds_read_b128 v[64:67], %[vxa] offset:288\n"
        "v_mfma_f32_16x16x32_bf16 %[a1], v[108:111], v[40:43], %[a1]\n"
        "ds_read_b128 v[60:63], %[vxa] offset:192\n"
        "s_waitcnt lgkmcnt(13)\n"
        "v_mfma_f32_16x16x32_bf16 %[a0], v[108:111], v[68:71], %[a0]\n"
        "ds_read_b128 v[100:103], %[vxb] offset:384\n"
        "s_branch EM%=\n"
        "SLOW0%=:\n"
        "s_mov_b32 %[sc], 0x100000\n"
        "SPIN0%=:\n"
        "ds_read_b32 %[vfl], %[vfa]\n"
        "s_sub_u32 %[sc], %[sc], 1\n"
        "s_cbranch_scc1 CONT0%=\n"
        "s_waitcnt lgkmcnt(0)\n"
        "v_readfirstlane_b32 %[st], %[vfl]\n"
        "s_andn2_b32 %[st], %[snd], %[st]\n"
        "s_cbranch_scc1 SPIN0%=\n"
        "s_branch CONT0%=\n"
        "SLOW1%=:\n"
        "s_mov_b32 %[sc], 0x100000\n"
        "SPIN1%=:\n"
        "ds_read_b32 %[vfl], %[vfa]\n"
        "s_sub_u32 %[sc], %[sc], 1\n"
        "s_cbranch_scc1 CONT1%=\n"
        "s_waitcnt lgkmcnt(0)\n"
        "v_readfirstlane_b32 %[st], %[vfl]\n"
        "s_andn2_b32 %[st], %[snd], %[st]\n"
        "s_cbranch_scc1 SPIN1%=\n"
        "s_branch CONT1%=\n"
        "L%=:\n"
        "s_waitcnt lgkmcnt(5)\n"
        "v_mfma_f32_16x16x32_bf16 %[a6], v[104:107], v[48:51], %[a6]\n"
        "ds_read_b64_tr_b16 v[112:113], %[vw] offset:8192\n"
        "v_mfma_f32_16x16x32_bf16 %[a5], v[104:107], v[44:47], %[a5]\n"
        "ds_read_b64_tr_b16 v[114:115], %[vw] offset:8448\n"
        "v_mfma_f32_16x16x32_bf16 %[a4], v[104:107], v[40:43], %[a4]\n"
        "ds_read_b128 v[48:51], %[vxa] offset:288\n"
        "v_mfma_f32_16x16x32_bf16 %[a3], v[104:107], v[68:71], %[a3]\n"
        "v_mfma_f32_16x16x32_bf16 %[a2], v[104:107], v[64:67], %[a2]\n"
        "v_mfma_f32_16x16x32_bf16 %[a1], v[104:107], v[60:63], %[a1]\n"
        "v_mfma_f32_16x16x32_bf16 %[a0], v[104:107], v[56:59], %[a0]\n"
        "s_waitcnt lgkmcnt(6)\n"
        "v_mfma_f32_16x16x32_bf16 %[a6], v[108:111], v[44:47], %[a6]\n"
        "ds_read_b64_tr_b16 v[104:105], %[vw] offset:4096\n"
        "v_mfma_f32_16x16x32_bf16 %[a5], v[108:111], v[40:43], %[a5]\n"
        "ds_read_b64_tr_b16 v[106:107], %[vw] offset:4352\n"
        "v_mfma_f32_16x16x32_bf16 %[a4], v[108:111], v[68:71], %[a4]\n"
        "ds_read_b128 v[44:47], %[vxa] offset:192\n"
        "v_mfma_f32_16x16x32_bf16 %[a3], v[108:111], v[64:67], %[a3]\n"
        "v_mfma_f32_16x16x32_bf16 %[a2], v[108:111], v[60:63], %[a2]\n"
        "v_mfma_f32_16x16x32_bf16 %[a1], v[108:111], v[56:59], %[a1]\n"
        "v_mfma_f32_16x16x32_bf16 %[a0], v[108:111], v[52:55], %[a0]\n"
        "s_waitcnt lgkmcnt(4)\n"
        "v_mfma_f32_16x16x32_bf16 %[a6], v[112:115], v[80:83], %[a6]\n"
        "ds_read_b64_tr_b16 v[108:109], %[vw] offset:6144\n"
        "v_mfma_f32_16x16x32_bf16 %[a5], v[112:115], v[76:79], %[a5]\n"
        "ds_read_b64_tr_b16 v[110:111], %[vw] offset:6400\n"
        "v_mfma_f32_16x16x32_bf16 %[a4], v[112:115], v[72:75], %[a4]\n"
        "ds_read_b128 v[80:83], %[vxb] offset:288\n"
        "v_mfma_f32_16x16x32_bf16 %[a3], v[112:115], v[100:103], %[a3]\n"
        "ds_read_b128 v[76:79], %[vxb] offset:192\n"
        "v_mfma_f32_16x16x32_bf16 %[a2], v[112:115], v[96:99], %[a2]\n"
        "v_mfma_f32_16x16x32_bf16 %[a1], v[112:115], v[92:95], %[a1]\n"
        "ds_read_b32 %[vfl], %[vfa]\n"
        "v_mfma_f32_16x16x32_bf16 %[a0], v[112:115], v[88:91], %[a0]\n"
        "s_waitcnt lgkmcnt(6)\n"
        "v_mfma_f32_16x16x32_bf16 %[a6], v[104:107], v[40:43], %[a6]\n"
        "ds_read_b64_tr_b16 v[112:113], %[vw] offset:10240\n"
        "v_mfma_f32_16x16x32_bf16 %[a5], v[104:107], v[68:71], %[a5]\n"
        "ds_read_b64_tr_b16 v[114:115], %[vw] offset:10496\n"
        "v_mfma_f32_16x16x32_bf16 %[a4], v[104:107], v[64:67], %[a4]\n"
        "ds_read_b128 v[40:43], %[vxa] offset:96\n"
        "v_mfma_f32_16x16x32_bf16 %[a3], v[104:107], v[60:63], %[a3]\n"
        "v_add_u32 %[vw], 0x3000, %[vw]\n"
        "v_mfma_f32_16x16x32_bf16 %[a2], v[104:107], v[56:59], %[a2]\n"
        "v_mfma_f32_16x16x32_bf16 %[a1], v[104:107], v[52:55], %[a1]\n"
        "v_mfma_f32_16x16x32_bf16 %[a0], v[104:107], v[48:51], %[a0]\n"
        "s_waitcnt lgkmcnt(3)\n"
        "v_mfma_f32_16x16x32_bf16 %[a6], v[108:111], v[68:71], %[a6]\n"
        "v_readfirstlane_b32 %[st], %[vfl]\n"
        "ds_read_b128 v[68:71], %[vxa] offset:0\n"
        "v_mfma_f32_16x16x32_bf16 %[a5], v[108:111], v[64:67], %[a5]\n"
        "s_andn2_b32 %[st], %[snd], %[st]\n"
        "s_cbranch_scc1 SLOW1%=\n"
        "CONT1%=:\n"
        "ds_read_b64_tr_b16 v[104:105], %[vw] offset:0\n"
        "v_mfma_f32_16x16x32_bf16 %[a4], v[108:111], v[60:63], %[a4]\n"
        "ds_read_b64_tr_b16 v[106:107], %[vw] offset:256\n"
        "v_mfma_f32_16x16x32_bf16 %[a3], v[108:111], v[56:59], %[a3]\n"
        "v_add_u32 %[vxa], 0xfffffe80, %[vxa]\n"
        "v_mfma_f32_16x16x32_bf16 %[a2], v[108:111], v[52:55], %[a2]\n"
        "s_lshl_b32 %[snd], %[snd], 1\n"
        "v_mfma_f32_16x16x32_bf16 %[a1], v[108:111], v[48:51], %[a1]\n"
        "v_mfma_f32_16x16x32_bf16 %[a0], v[108:111], v[44:47], %[a0]\n"
        "s_waitcnt lgkmcnt(4)\n"
        "v_mfma_f32_16x16x32_bf16 %[a6], v[112:115], v[72:75], %[a6]\n"
        "ds_read_b64_tr_b16 v[108:109], %[vw] offset:2048\n"
        "v_mfma_f32_16x16x32_bf16 %[a5], v[112:115], v[100:103], %[a5]\n"
        "ds_read_b64_tr_b16 v[110:111], %[vw] offset:2304\n"
        "v_mfma_f32_16x16x32_bf16 %[a4], v[112:115], v[96:99], %[a4]\n"
        "ds_read_b128 v[72:75], %[vxb] offset:96\n"
        "v_mfma_f32_16x16x32_bf16 %[a3], v[112:115], v[92:95], %[a3]\n"
        "ds_read_b128 v[100:103], %[vxb] offset:0\n"
        "v_mfma_f32_16x16x32_bf16 %[a2], v[112:115], v[88:91], %[a2]\n"
        "ds_write_b32 %[vpa], %[vw]\n"
        "v_add_u32 %[vxb], 0xfffffe80, %[vxb]\n"
        "v_mfma_f32_16x16x32_bf16 %[a1], v[112:115], v[84:87], %[a1]\n"
        "v_mfma_f32_16x16x32_bf16 %[a0], v[112:115], v[80:83], %[a0]\n"
        "s_waitcnt lgkmcnt(5)\n"
        "v_mfma_f32_16x16x32_bf16 %[a6], v[104:107], v[64:67], %[a6]\n"
        "ds_read_b64_tr_b16 v[112:113], %[vw] offset:8192\n"
        "v_mfma_f32_16x16x32_bf16 %[a5], v[104:107], v[60:63], %[a5]\n"
        "ds_read_b64_tr_b16 v[114:115], %[vw] offset:8448\n"
        "v_mfma_f32_16x16x32_bf16 %[a4], v[104:107], v[56:59], %[a4]\n"
        "ds_read_b128 v[64:67], %[vxa] offset:288\n"
        "v_mfma_f32_16x16x32_bf16 %[a3], v[104:107], v[52:55], %[a3]\n"
        "v_mfma_f32_16x16x32_bf16 %[a2], v[104:107], v[48:51], %[a2]\n"
        "v_mfma_f32_16x16x32_bf16 %[a1], v[104:107], v[44:47], %[a1]\n"
        "v_mfma_f32_16x16x32_bf16 %[a0], v[104:107], v[40:43], %[a0]\n"
        "s_waitcnt lgkmcnt(6)\n"
        "v_mfma_f32_16x16x32_bf16 %[a6], v[108:111], v[60:63], %[a6]\n"
        "ds_read_b64_tr_b16 v[104:105], %[vw] offset:4096\n"
        "v_mfma_f32_16x16x32_bf16 %[a5], v[108:111], v[56:59], %[a5]\n"
        "ds_read_b64_tr_b16 v[106:107], %[vw] offset:4352\n"
        "v_mfma_f32_16x16x32_bf16 %[a4], v[108:111], v[52:55], %[a4]\n"
        "ds_read_b128 v[60:63], %[vxa] offset:192\n"
        "v_mfma_f32_16x16x32_bf16 %[a3], v[108:111], v[48:51], %[a3]\n"
        "v_mfma_f32_16x16x32_bf16 %[a2], v[108:111], v[44:47], %[a2]\n"
        "v_mfma_f32_16x16x32_bf16 %[a1], v[108:111], v[40:43], %[a1]\n"
        "v_mfma_f32_16x16x32_bf16 %[a0], v[108:111], v[68:71], %[a0]\n"
        "EM%=:\n"
        "s_waitcnt lgkmcnt(4)\n"
        "v_mfma_f32_16x16x32_bf16 %[a6], v[112:115], v[96:99], %[a6]\n"
        "ds_read_b64_tr_b16 v[108:109], %[vw] offset:6144\n"
        "v_mfma_f32_16x16x32_bf16 %[a5], v[112:115], v[92:95], %[a5]\n"
        "ds_read_b64_tr_b16 v[110:111], %[vw] offset:6400\n"
        "v_mfma_f32_16x16x32_bf16 %[a4], v[112:115], v[88:91], %[a4]\n"
        "ds_read_b128 v[96:99], %[vxb] offset:288\n"
        "v_mfma_f32_16x16x32_bf16 %[a3], v[112:115], v[84:87], %[a3]\n"
        "ds_read_b128 v[92:95], %[vxb] offset:192\n"
        "v_mfma_f32_16x16x32_bf16 %[a2], v[112:115], v[80:83], %[a2]\n"
        "v_mfma_f32_16x16x32_bf16 %[a1], v[112:115], v[76:79], %[a1]\n"
        "ds_read_b32 %[vfl], %[vfa]\n"
        "v_mfma_f32_16x16x32_bf16 %[a0], v[112:115], v[72:75], %[a0]\n"
        "s_waitcnt lgkmcnt(6)\n"
        "v_mfma_f32_16x16x32_bf16 %[a6], v[104:107], v[56:59], %[a6]\n"
        "ds_read_b64_tr_b16 v[112:113], %[vw] offset:10240\n"
        "v_mfma_f32_16x16x32_bf16 %[a5], v[104:107], v[52:55], %[a5]\n"
        "ds_read_b64_tr_b16 v[114:115], %[vw] offset:10496\n"
        "v_mfma_f32_16x16x32_bf16 %[a4], v[104:107], v[48:51], %[a4]\n"
        "ds_read_b128 v[56:59], %[vxa] offset:96\n"
        "v_mfma_f32_16x16x32_bf16 %[a3], v[104:107], v[44:47], %[a3]\n"
        "v_add_u32 %[vw], 0x3000, %[vw]\n"
        "v_mfma_f32_16x16x32_bf16 %[a2], v[104:107], v[40:43], %[a2]\n"
        "v_mfma_f32_16x16x32_bf16 %[a1], v[104:107], v[68:71], %[a1]\n"
        "v_mfma_f32_16x16x32_bf16 %[a0], v[104:107], v[64:67], %[a0]\n"
        "s_waitcnt lgkmcnt(3)\n"
        "v_mfma_f32_16x16x32_bf16 %[a6], v[108:111], v[52:55], %[a6]\n"
        "v_readfirstlane_b32 %[st], %[vfl]\n"
        "ds_read_b128 v[52:55], %[vxa] offset:0\n"
        "v_mfma_f32_16x16x32_bf16 %[a5], v[108:111], v[48:51], %[a5]\n"
        "s_andn2_b32 %[st], %[snd], %[st]\n"
        "s_cbranch_scc1 SLOW0%=\n"
        "CONT0%=:\n"
        "ds_read_b64_tr_b16 v[104:105], %[vw] offset:0\n"
        "v_mfma_f32_16x16x32_bf16 %[a4], v[108:111], v[44:47], %[a4]\n"
        "ds_read_b64_tr_b16 v[106:107], %[vw] offset:256\n"
        "v_mfma_f32_16x16x32_bf16 %[a3], v[108:111], v[40:43], %[a3]\n"
        "v_add_u32 %[vxa], 0xfffffe80, %[vxa]\n"
        "v_mfma_f32_16x16x32_bf16 %[a2], v[108:111], v[68:71], %[a2]\n"
        "s_lshl_b32 %[snd], %[snd], 1\n"
        "v_mfma_f32_16x16x32_bf16 %[a1], v[108:111], v[64:67], %[a1]\n"
        "v_mfma_f32_16x16x32_bf16 %[a0], v[108:111], v[60:63], %[a0]\n"
        "s_waitcnt lgkmcnt(4)\n"
        "v_mfma_f32_16x16x32_bf16 %[a6], v[112:115], v[88:91], %[a6]\n"
        "ds_read_b64_tr_b16 v[108:109], %[vw] offset:2048\n"
        "v_mfma_f32_16x16x32_bf16 %[a5], v[112:115], v[84:87], %[a5]\n"
        "ds_read_b64_tr_b16 v[110:111], %[vw] offset:2304\n"
        "v_mfma_f32_16x16x32_bf16 %[a4], v[112:115], v[80:83], %[a4]\n"
        "ds_read_b128 v[88:91], %[vxb] offset:96\n"
        "v_mfma_f32_16x16x32_bf16 %[a3], v[112:115], v[76:79], %[a3]\n"
        "ds_read_b128 v[84:87], %[vxb] offset:0\n"
        "v_mfma_f32_16x16x32_bf16 %[a2], v[112:115], v[72:75], %[a2]\n"
        "ds_write_b32 %[vpa], %[vw]\n"
        "v_add_u32 %[vxb], 0xfffffe80, %[vxb]\n"
        "v_mfma_f32_16x16x32_bf16 %[a1], v[112:115], v[100:103], %[a1]\n"
        "s_bitcmp0_b32 %[snd], 8\n"
        "v_mfma_f32_16x16x32_bf16 %[a0], v[112:115], v[96:99], %[a0]\n"
        "s_cbranch_scc1 L%=\n"
        "s_waitcnt lgkmcnt(5)\n"
        "v_mfma_f32_16x16x32_bf16 %[a6], v[104:107], v[48:51], %[a6]\n"
        "ds_read_b64_tr_b16 v[112:113], %[vw] offset:8192\n"
        "v_mfma_f32_16x16x32_bf16 %[a5], v[104:107], v[44:47], %[a5]\n"
        "ds_read_b64_tr_b16 v[114:115], %[vw] offset:8448\n"
        "v_mfma_f32_16x16x32_bf16 %[a4], v[104:107], v[40:43], %[a4]\n"
        "ds_read_b128 v[48:51], %[vxa] offset:288\n"
        "v_mfma_f32_16x16x32_bf16 %[a3], v[104:107], v[68:71], %[a3]\n"
        "v_mfma_f32_16x16x32_bf16 %[a2], v[104:107], v[64:67], %[a2]\n"
        "v_mfma_f32_16x16x32_bf16 %[a1], v[104:107], v[60:63], %[a1]\n"
        "v_mfma_f32_16x16x32_bf16 %[a0], v[104:107], v[56:59], %[a0]\n"
        "s_waitcnt lgkmcnt(6)\n"
        "v_mfma_f32_16x16x32_bf16 %[a6], v[108:111], v[44:47], %[a6]\n"
        "ds_read_b64_tr_b16 v[104:105], %[vw] offset:4096\n"
        "v_mfma_f32_16x16x32_bf16 %[a5], v[108:111], v[40:43], %[a5]\n"
        "ds_read_b64_tr_b16 v[106:107], %[vw] offset:4352\n"
        "v_mfma_f32_16x16x32_bf16 %[a4], v[108:111], v[68:71], %[a4]\n"
        "v_mfma_f32_16x16x32_bf16 %[a3], v[108:111], v[64:67], %[a3]\n"
        "v_mfma_f32_16x16x32_bf16 %[a2], v[108:111], v[60:63], %[a2]\n"
        "v_mfma_f32_16x16x32_bf16 %[a1], v[108:111], v[56:59], %[a1]\n"
        "v_mfma_f32_16x16x32_bf16 %[a0], v[108:111], v[52:55], %[a0]\n"
        "s_waitcnt lgkmcnt(3)\n"
        "v_mfma_f32_16x16x32_bf16 %[a6], v[112:115], v[80:83], %[a6]\n"
        "ds_read_b64_tr_b16 v[108:109], %[vw] offset:10240\n"
        "v_mfma_f32_16x16x32_bf16 %[a5], v[112:115], v[76:79], %[a5]\n"
        "ds_read_b64_tr_b16 v[110:111], %[vw] offset:10496\n"
        "v_mfma_f32_16x16x32_bf16 %[a4], v[112:115], v[72:75], %[a4]\n"
        "ds_read_b128 v[80:83], %[vxb] offset:288\n"
        "v_mfma_f32_16x16x32_bf16 %[a3], v[112:115], v[100:103], %[a3]\n"
        "v_mfma_f32_16x16x32_bf16 %[a2], v[112:115], v[96:99], %[a2]\n"
        "v_mfma_f32_16x16x32_bf16 %[a1], v[112:115], v[92:95], %[a1]\n"
        "v_mfma_f32_16x16x32_bf16 %[a0], v[112:115], v[88:91], %[a0]\n"
        "s_waitcnt lgkmcnt(3)\n"
        "v_mfma_f32_16x16x32_bf16 %[a6], v[104:107], v[40:43], %[a6]\n"
        "v_mfma_f32_16x16x32_bf16 %[a5], v[104:107], v[68:71], %[a5]\n"
        "v_mfma_f32_16x16x32_bf16 %[a4], v[104:107], v[64:67], %[a4]\n"
        "v_mfma_f32_16x16x32_bf16 %[a3], v[104:107], v[60:63], %[a3]\n"
        "v_mfma_f32_16x16x32_bf16 %[a2], v[104:107], v[56:59], %[a2]\n"
        "v_mfma_f32_16x16x32_bf16 %[a1], v[104:107], v[52:55], %[a1]\n"
        "v_mfma_f32_16x16x32_bf16 %[a0], v[104:107], v[48:51], %[a0]\n"
        "s_waitcnt lgkmcnt(1)\n"
        "v_mfma_f32_16x16x32_bf16 %[a6], v[108:111], v[72:75], %[a6]\n"
        "v_mfma_f32_16x16x32_bf16 %[a5], v[108:111], v[100:103], %[a5]\n"
        "v_mfma_f32_16x16x32_bf16 %[a4], v[108:111], v[96:99], %[a4]\n"
        "v_mfma_f32_16x16x32_bf16 %[a3], v[108:111], v[92:95], %[a3]\n"
        "v_mfma_f32_16x16x32_bf16 %[a2], v[108:111], v[88:91], %[a2]\n"
        "v_mfma_f32_16x16x32_bf16 %[a1], v[108:111], v[84:87], %[a1]\n"
        "s_waitcnt lgkmcnt(0)\n"
        "v_mfma_f32_16x16x32_bf16 %[a0], v[108:111], v[80:83], %[a0]\n"
        "s_setprio 0\n"
        "s_nop 6\n"
        : [a0] "+v"(a0), [a1] "+v"(a1), [a2] "+v"(a2), [a3] "+v"(a3), [a4] "+v"(a4), [a5] "+v"(a5), [a6] "+v"(a6),
          [vw] "+v"(vw), [vxa] "+v"(vxa), [vxb] "+v"(vxb), [vfl] "=&v"(vfl), [snd] "+s"(snd),
          [st] "=&s"(st), [sc] "=&s"(sc), "+v"(pb1), "+v"(pb2)
        : [vfa] "v"(vfa), [vpa] "v"(vpa)
        : "memory", "scc",
          "v40", "v41", "v42", "v43", "v44", "v45", "v46", "v47", "v48", "v49", "v50", "v51", "v52", "v53", "v54", "v55", "v56", "v57", "v58", "v59", "v60", "v61", "v62", "v63", "v64", "v65", "v66", "v67", "v68", "v69", "v70", "v71", "v72", "v73", "v74", "v75", "v76", "v77", "v78", "v79", "v80", "v81", "v82", "v83", "v84", "v85", "v86", "v87", "v88", "v89", "v90", "v91", "v92", "v93", "v94", "v95", "v96", "v97", "v98", "v99", "v100", "v101", "v102", "v103", "v104", "v105", "v106", "v107", "v108", "v109", "v110", "v111", "v112", "v113", "v114", "v115"
      );
      STAMP(2);
      auto rp = __builtin_amdgcn_make_buffer_rsrc((void*)(ws + WS_PART), 0, PART_BYTES, 0x00020000);
      const f32x4 acc[8] = {a0, a1, a2, a3, a4, a5, a6, a6};
      auto pk = [](float a, float b) -> unsigned { const unsigned u = pk_bf16(a, b); return u < ~1u ? u : ~1u; };
#pragma unroll
      for (int jp = 3; jp >= 0; --jp) {
        __builtin_amdgcn_sched_barrier(0);
        auto s0 = __builtin_amdgcn_permlane16_swap(pk(acc[2 * jp][0], acc[2 * jp][1]), pk(acc[2 * jp + 1][0], acc[2 * jp + 1][1]), false, false);
        auto s1 = __builtin_amdgcn_permlane16_swap(pk(acc[2 * jp][2], acc[2 * jp][3]), pk(acc[2 * jp + 1][2], acc[2 * jp + 1][3]), false, false);
        u32x4 v = {s0[0], s1[0], s0[1], s1[1]};
        __builtin_amdgcn_raw_buffer_store_b128(v, rp, jp == 3 ? pb2 : pb1, jp * (8 * 27 * 512), 16);
      }
      __builtin_amdgcn_sched_barrier(0);
    }
    STAMP(9);
  }
  const int q = lane >> 4;
  unsigned go = (m * 28 + c) * 27 * 512 + wid * 256 + lane * 4;
  unsigned wa = lds0 + L_W + (8 * q + ((lane & 15) >> 2)) * 288 + (lane & 3) * 8 + wid * 32;
  u32x4 srd = {kb[0] + WS_PART, kb[1], PART_BYTES, 0x00020000};
  srd[1] = (srd[1] + (srd[0] < WS_PART)) & 0xffff;
  asm volatile("s_barrier" : "+v"(go), "+v"(wa), "+s"(srd) :: "memory");
  STAMP(4);
  s16x8 oa[5];
#pragma unroll
  for (int s = 0; s < 5; ++s) {
    const unsigned a = s < 4 ? wa + s * 32 * 288 : wa + 128 * 288 - (q >> 1) * 16 * 288;
    s16x4 lo = __builtin_amdgcn_ds_read_tr16_b64_v4i16(LDSP(s16x4, a));
    s16x4 hi = __builtin_amdgcn_ds_read_tr16_b64_v4i16(LDSP(s16x4, a + 4 * 288));
    oa[s] = (s16x8)__builtin_shufflevector(lo, hi, 0, 1, 2, 3, 4, 5, 6, 7);
  }
  auto ro = __builtin_amdgcn_make_buffer_rsrc((void*)mkptr(ka[6], ka[7]), 0, X_BYTES, 0x00020000);
  const int rho = lane & 7;
  int rr = rA + 8 * (c & 3) + rho;
  int sq = sA;
  if (rr >= 72) { rr -= 72; sq += 1; }
  const int p = rr * 7 + (c >> 2);
  unsigned oo = (p < 500 && !(lane & 8)) ? ((sq >> 1) * 1000 + 2 * p + (sq & 1)) * 288 + q * 8 + wid * 32 : 0x80000000u;
  unsigned za = lds0 + L_Z + rho * 336 + q * 16;
  unsigned zw = lds0 + L_Z + ((lane >> 2) & 7) * 336 + wid * 32 + (lane >> 5) * 16 + (lane & 3) * 4;
  unsigned r0, r1, r2, r3, r4, r5, t0, t1, cnt;
#define GLD(r, so, off) "buffer_load_dword %[" r "], %[go], %[srd], " so " offen offset:" off " sc1\n"
#define GRE(r, so, off) "v_cmp_eq_u32 vcc, -1, %[" r "]\n s_mov_b64 exec, vcc\n" GLD(r, so, off) "s_mov_b64 exec, -1\n"
#define STR2(x) #x
#define STR(x) STR2(x)
  asm volatile(
      "s_sleep " STR(SWEEP_DELAY) "\n"
      GLD("r0", "0", "0") GLD("r1", "%[o1]", "0") GLD("r2", "%[o2]", "0")
      GLD("r3", "0", "2304") GLD("r4", "%[o1]", "2304") GLD("r5", "%[o2]", "2304")
      "s_mov_b32 %[cnt], -1\n"
      "s_waitcnt vmcnt(0)\n"
      "CHK%=:\n"
      "v_max3_u32 %[t0], %[r0], %[r1], %[r2]\n"
      "v_max3_u32 %[t1], %[r3], %[r4], %[r5]\n"
      "v_max_u32 %[t0], %[t0], %[t1]\n"
      "v_cmp_eq_u32 vcc, -1, %[t0]\n"
      "s_cbranch_vccz FIN%=\n"
      "s_add_u32 %[cnt], %[cnt], 1\n"
      "s_and_b32 vcc_lo, %[cnt], " STR(RETRY_MASK) "\n"
      "s_cbranch_scc1 SLP%=\n"
      GRE("r0", "0", "0") GRE("r1", "%[o1]", "0") GRE("r2", "%[o2]", "0")
      GRE("r3", "0", "2304") GRE("r4", "%[o1]", "2304") GRE("r5", "%[o2]", "2304")
      "SLP%=:\n"
      "s_sleep " STR(RETRY_SLEEP) "\n"
      "s_bitcmp0_b32 %[cnt], 22\n"
      "s_cbranch_scc1 CHK%=\n"
      "FIN%=:\n"
      : [r0] "=&v"(r0), [r1] "=&v"(r1), [r2] "=&v"(r2), [r3] "=&v"(r3), [r4] "=&v"(r4), [r5] "=&v"(r5),
        [t0] "=&v"(t0), [t1] "=&v"(t1), [cnt] "=&s"(cnt), "+v"(oo), "+v"(za), "+v"(zw)
      : [go] "v"(go), [srd] "s"(srd), [o1] "s"(9 * 512), [o2] "s"(18 * 512)
      : "memory", "vcc", "scc");
  STAMP(5);
  {
    float zl = gate(bf_lo(r0) + bf_lo(r1) + bf_lo(r2), bf_lo(r3) + bf_lo(r4) + bf_lo(r5));
    float zh = gate(bf_hi(r0) + bf_hi(r1) + bf_hi(r2), bf_hi(r3) + bf_hi(r4) + bf_hi(r5));
    *LDSP(unsigned, zw) = pk_bf16(zl, zh);
  }
  STAMP(6);
  asm volatile("s_waitcnt lgkmcnt(0)\n\ts_barrier" ::: "memory");
  s16x8 zf[5];
#pragma unroll
  for (int s = 0; s < 5; ++s) zf[s] = ld_frag(za + s * 64);
  __builtin_amdgcn_sched_barrier(0);
  f32x4 ra = {0.f, 0.f, 0.f, 0.f}, rb = ra;
  ra = MFMA(oa[0], zf[0], ra); rb = MFMA(oa[1], zf[1], rb);
  ra = MFMA(oa[2], zf[2], ra); rb = MFMA(oa[3], zf[3], rb);
  ra = MFMA(oa[4], zf[4], ra);
  ra += rb;
  u32x2 va = {pk_bf16(ra[0], ra[1]), pk_bf16(ra[2], ra[3])};
  __builtin_amdgcn_raw_buffer_store_b64(va, ro, oo, 0, 17);
  asm volatile("" :: "v"(r0), "v"(r1), "v"(r2), "v"(r3), "v"(r4), "v"(r5) : "memory");
  STAMP(7);
}

extern "C" void kernel_launch(void* const* d_in, const int* in_sizes, int n_in, void* d_out, int out_size, void* d_ws,
                              size_t ws_size, hipStream_t stream) {
  wavenet_kernel<<<252, 576, 0, stream>>>((const char*)d_in[0], (const char*)d_in[1], (const char*)d_in[2], (char*)d_out,
                                          (char*)d_ws);
}
```
